```python
import torch

def _softmax(x, axis=-1):
    return torch.softmax(x, dim=axis)

def _gelu(x):
    return torch.nn.functional.gelu(x, approximate='tanh')

def _silu(x):
    return torch.nn.functional.silu(x)

def _sigmoid(x):
    return torch.sigmoid(x)

def _layernorm(x, axis=-1, eps=1e-05):
    m = x.mean(dim=axis, keepdim=True)
    v = x.var(dim=axis, keepdim=True, unbiased=False)
    return (x - m) / torch.sqrt(v + eps)

def _scatter_add(tgt, idx, src, axis=0):
    return tgt.clone().index_add_(axis, idx, src)

def _segment_sum(src, seg, n):
    out = torch.zeros((n,) + src.shape[1:], dtype=src.dtype)
    return out.index_add_(0, seg, src)

def _pad(x, pw):
    flat = tuple((v for pair in reversed(pw) for v in pair))
    return torch.nn.functional.pad(x, flat)
kernel_name = 'standalone__window_attn_davit__vision'

def setup_inputs():

    _w = lambda a: (lambda t: t.to(torch.bfloat16) if t.is_floating_point() else t)(torch.as_tensor(a))
    HW = 224
    C0 = 192
    ws = 7
    nh = 4
    H = W = max(HW // ws * ws, ws)
    C = max(C0 // nh * nh, nh)
    k = 3
    return {'x': _w((lambda *s: torch.randn(*s, dtype=torch.bfloat16) * 1.6)(1, H, W, C)), 'dw_pe': _w((lambda *s: torch.randn(*s, dtype=torch.bfloat16) * 1.6)(k, k, C) * 0.1), 'w_qkv': _w((lambda *s: torch.randn(*s, dtype=torch.bfloat16) * 1.6)(C, 3 * C) * C ** (-0.5)), 'w_o': _w((lambda *s: torch.randn(*s, dtype=torch.bfloat16) * 0.4)(C, C) * C ** (-0.5))}

def reference(x, dw_pe, w_qkv, w_o):
    B, H, W, C = x.shape
    ws = 7
    nh = 4
    dh = C // nh
    kk = dw_pe.shape[0]
    pad = kk // 2
    xp = _pad(x, ((0, 0), (pad, pad), (pad, pad), (0, 0)))
    pe = torch.zeros_like(x)
    for i in range(kk):
        for j in range(kk):
            pe = pe + xp[:, i:i + H, j:j + W, :] * dw_pe[i, j][None, None, None, :]
    x0 = x + pe
    h = _layernorm(x0, axis=-1)
    hw = h.reshape(B, H // ws, ws, W // ws, ws, C)
    hw = (lambda x, *d: x.permute(*d))(hw, 0, 1, 3, 2, 4, 5)
    hw = hw.reshape(-1, ws * ws, C)
    qkv = (hw @ w_qkv).reshape(-1, ws * ws, 3, nh, dh)
    q = qkv[:, :, 0].clone()
    k = qkv[:, :, 1].clone()
    v = qkv[:, :, 2].clone()
    scale = dh ** (-0.5)
    attn = torch.einsum('wnhd,wmhd->whnm', q, k) * scale
    attn = _softmax(attn, axis=-1)
    out = torch.einsum('whnm,wmhd->wnhd', attn, v)
    out = out.reshape(-1, ws * ws, C) @ w_o
    out = out.reshape(B, H // ws, W // ws, ws, ws, C)
    out = (lambda x, *d: x.permute(*d))(out, 0, 1, 3, 2, 4, 5)
    out = out.reshape(B, H, W, C)
    return x0 + out
```

```cpp
#include <hip/hip_runtime.h>

static __device__ const __attribute__((aligned(1024))) unsigned short kTab[512] = {
  2304,8448,14592,20736,35072,47360,53504,5376,5760,11904,18048,32384,38528,50816,5760,11904,
  0,12288,18432,32768,38912,45056,0,12288,384,12672,18816,33152,39296,45440,384,12672,
  768,6912,19200,33536,39680,45824,51968,3840,1152,7296,19584,36992,43136,49280,1152,7296,
  1536,7680,13824,31232,43520,49664,1536,7680,1920,8064,14208,31616,43904,50048,1920,8064,
  5376,11520,17664,32000,38144,50432,2304,8448,8832,14976,21120,35456,41600,53888,8832,14976,
  9216,15360,21504,35840,41984,54272,9216,15360,3456,15744,21888,36224,42368,48512,3456,15744,
  3840,16128,22272,36608,42752,48896,768,6912,4224,10368,22656,40064,46208,52352,4224,10368,
  4608,10752,23040,40448,46592,52736,4608,10752,4992,11136,17280,34688,46976,53120,4992,11136,
  11520,17664,32000,38144,50432,0,0,0,18048,32384,38528,50816,53888,0,0,0,
  18432,32768,38912,45056,54272,0,0,0,18816,33152,39296,45440,48512,0,0,0,
  16128,22272,36608,42752,48896,0,0,0,19584,36992,43136,49280,52352,0,0,0,
  13824,31232,43520,49664,52736,0,0,0,14208,31616,43904,50048,53120,0,0,0,
  14592,20736,35072,47360,53504,0,0,0,21120,35456,41600,53888,53888,0,0,0,
  21504,35840,41984,54272,54272,0,0,0,21888,36224,42368,48512,48512,0,0,0,
  19200,33536,39680,45824,51968,0,0,0,22656,40064,46208,52352,52352,0,0,0,
  23040,40448,46592,52736,52736,0,0,0,17280,34688,46976,53120,53120,0,0,0,
  6,229,452,675,898,1121,1344,9,232,455,909,1132,1355,20,243,466,
  689,912,1135,1358,23,246,469,923,1146,1369,65535,65535,65535,65535,65535,65535,
  230,453,676,899,1122,1345,10,233,456,679,1133,1356,244,467,690,913,
  1136,1359,24,247,470,693,1147,1370,65535,65535,65535,65535,65535,65535,65535,65535,
  0,454,677,900,1123,1346,11,234,457,680,903,1357,14,468,691,914,
  1137,1360,25,248,471,694,917,1371,65535,65535,65535,65535,65535,65535,65535,65535,
  1,224,678,901,1124,1347,12,235,458,681,904,1127,15,238,692,915,
  1138,1361,26,249,472,695,918,1141,65535,65535,65535,65535,65535,65535,65535,65535,
  2,225,448,902,1125,1348,13,236,459,682,905,1128,1351,16,239,462,
  916,1139,1362,27,250,473,696,919,1142,1365,65535,65535,65535,65535,65535,65535,
  3,226,449,672,1126,1349,237,460,683,906,1129,1352,17,240,463,686,
  1140,1363,251,474,697,920,1143,1366,65535,65535,65535,65535,65535,65535,65535,65535,
  4,227,450,673,896,1350,7,461,684,907,1130,1353,18,241,464,687,
  910,1364,21,475,698,921,1144,1367,65535,65535,65535,65535,65535,65535,65535,65535,
  5,228,451,674,897,1120,8,231,685,908,1131,1354,19,242,465,688,
  911,1134,22,245,699,922,1145,1368,65535,65535,65535,65535,65535,65535,65535,65535
};

__global__ __launch_bounds__(256, 1) void davit_kernel(const char* __restrict__ x, const char* __restrict__ dw, const char* __restrict__ wq,
                                                        const char* __restrict__ wo, char* __restrict__ out, char* __restrict__ ws) {
  __shared__ __attribute__((aligned(1024))) char smem[163840];
  asm volatile("" ::"s"((unsigned)(size_t)(__attribute__((address_space(3))) char*)smem) : "memory");

  unsigned tid = threadIdx.x;
  const unsigned long long pt = (unsigned long long)kTab;
  asm volatile(
  "s_load_dwordx8 s[4:11], %2, 0x0\n"
  "s_load_dwordx4 s[12:15], %2, 0x20\n"
  "v_readfirstlane_b32 s40, %0\n"
  "s_lshr_b32 s90, %7, 3\n"
  "s_getpc_b64 s[36:37]\n"
  "90:\n"
  "v_and_b32 v2, 63, %0\n"
  "s_and_b32 s37, s37, 0xffff\n"
  "s_mov_b32 s38, 91f-90b\n"
  "s_mov_b32 s39, 0x20000\n"
  "v_lshlrev_b32 v3, 7, v2\n"
  "s_lshl_b32 s82, s90, 13\n"
  "s_lshr_b32 s40, s40, 6\n"
  "buffer_load_dword v4, v3, s[36:39], s82 offen\n"
  "s_and_b32 s82, %7, 7\n"
  "s_lshl_b32 s82, s82, 2\n"
  "s_lshr_b32 s83, s90, 3\n"
  "s_add_u32 s82, s82, s83\n"
  "s_mul_i32 s89, s82, 7\n"
  "s_and_b32 s83, s90, 7\n"
  "s_mul_i32 s88, s83, 28\n"
  "v_mul_u32_u24 v253, 2731, v0\n"
  "v_lshrrev_b32 v253, 16, v253\n"
  "v_lshlrev_b32 v255, 4, v0\n"
  "v_lshlrev_b32 v254, 4, v253\n"
  "s_mul_i32 s83, s89, 224\n"
  "s_add_u32 s83, s83, s88\n"
  "s_mul_i32 s41, s83, 384\n"
  "s_sub_u32 s44, s41, 82560\n"
  "s_add_u32 s45, s41, 0\n"
  "s_add_u32 s46, s41, 89472\n"
  "s_add_u32 s47, s41, 172032\n"
  "s_add_u32 s48, s41, 261504\n"
  "s_add_u32 s49, s41, 344064\n"
  "s_add_u32 s50, s41, 433536\n"
  "s_add_u32 s51, s41, 516096\n"
  "s_add_u32 s52, s41, 605568\n"
  "s_cmp_eq_u32 s89, 0\n"
  "s_cselect_b32 s44, 0x40000000, s44\n"
  "s_cmp_eq_u32 s89, 217\n"
  "s_cselect_b32 s52, 0x40000000, s52\n"
  "s_cmp_eq_u32 s40, 3\n"
  "s_cselect_b32 s54, 0xffffff, -1\n"
  "s_cselect_b32 s55, 0, -1\n"
  "s_lshl_b32 s81, s40, 10\n"
  "s_movk_i32 s91, 0x70\n"
  "s_movk_i32 s42, 384\n"
  "s_cmp_eq_u32 s88, 0\n"
  "s_cselect_b32 s84, 0, -1\n"
  "v_cmp_eq_u32 vcc, s84, v254\n"
  "v_bfrev_b32 v253, 1\n"
  "v_add_u32 v252, 0, v254\n"
  "v_cndmask_b32_e64 v251, v255, v253, vcc\n"
  "v_bitop3_b32 v250, v251, v252, s91 bitop3:0x78\n"
  "v_add_u32 v249, 16, v254\n"
  "v_bitop3_b32 v248, v251, v249, s91 bitop3:0x78\n"
  "v_add_u32 v253, 32, v254\n"
  "v_bitop3_b32 v252, v251, v253, s91 bitop3:0x78\n"
  "v_add_u32 v249, 48, v254\n"
  "v_bitop3_b32 v247, v251, v249, s91 bitop3:0x78\n"
  "v_add_u32 v253, 64, v254\n"
  "v_bitop3_b32 v246, v251, v253, s91 bitop3:0x78\n"
  "v_add_u32 v249, 80, v254\n"
  "v_bitop3_b32 v245, v251, v249, s91 bitop3:0x78\n"
  "v_add_u32 v253, 96, v254\n"
  "v_bitop3_b32 v244, v251, v253, s91 bitop3:0x78\n"
  "v_add_u32 v249, 112, v254\n"
  "v_bitop3_b32 v243, v251, v249, s91 bitop3:0x78\n"
  "v_add_u32 v253, 0, v254\n"
  "v_bitop3_b32 v242, v251, v253, s91 bitop3:0x78\n"
  "v_lshrrev_b32 v249, 4, v2\n"
  "v_and_b32 v241, 15, v2\n"
  "s_lshl_b32 s86, s40, 5\n"
  "v_lshl_add_u32 v253, v241, 1, s86\n"
  "v_add_u32 v251, 1, v249\n"
  "v_mad_u32_u24 v240, v251, s42, v253\n"
  "v_add_u32 v239, 1152, v240\n"
  "v_lshrrev_b32 v238, 1, v249\n"
  "v_mul_u32_u24 v251, 3072, v238\n"
  "v_add_u32 v237, v251, v253\n"
  "v_lshlrev_b32 v236, 4, v2\n"
  "s_waitcnt lgkmcnt(0)\n"
  "s_sub_u32 s20, s4, 3840\n"
  "s_subb_u32 s21, s5, 0\n"
  "s_and_b32 s21, s21, 0xffff\n"
  "s_mov_b32 s22, 19271424\n"
  "s_mov_b32 s23, 0x20000\n"
  "global_load_ushort v235, v240, s[6:7] offset:0\n"
  "global_load_ushort v234, v239, s[6:7] offset:0\n"
  "global_load_ushort v233, v237, s[6:7] offset:0\n"
  "v_lshlrev_b32 v253, 1, v249\n"
  "global_load_ushort v232, v240, s[6:7] offset:128\n"
  "v_and_b32 v231, 7, v2\n"
  "global_load_ushort v230, v239, s[6:7] offset:128\n"
  "v_xor_b32 v229, v253, v231\n"
  "global_load_ushort v228, v237, s[6:7] offset:128\n"
  "v_lshlrev_b32 v229, 4, v229\n"
  "global_load_ushort v227, v240, s[6:7] offset:256\n"
  "v_lshrrev_b32 v226, 3, v2\n"
  "s_movk_i32 s85, 1152\n"
  "global_load_ushort v240, v239, s[6:7] offset:256\n"
  "v_mad_u32_u24 v199, v226, s85, v229\n"
  "global_load_ushort v225, v237, s[6:7] offset:256\n"
  "s_add_u32 m0, s81, 0\n"
  "v_lshlrev_b32 v251, 4, v241\n"
  "s_mov_b64 exec, s[54:55]\n"
  "buffer_load_dwordx4 v250, s[20:23], s44 offen nt lds\n"
  "buffer_load_dwordx4 v248, s[20:23], s45 offen offset:3456 nt lds\n"
  "s_mov_b64 exec, -1\n"
  "s_add_u32 m0, s81, 6912\n"
  "v_add_u32 v253, 86912, v251\n"
  "v_and_b32 v239, 1, v249\n"
  "v_mad_u32_u24 v248, v238, 20, 0\n"
  "v_add3_u32 v237, v248, v241, 6\n"
  "v_and_b32 v237, 7, v237\n"
  "v_lshrrev_b32 v251, 2, v241\n"
  "v_cmp_eq_u32 vcc, 0, v251\n"
  "v_lshrrev_b32 v250, 3, v241\n"
  "v_xor_b32 v250, v250, v239\n"
  "s_mov_b64 exec, s[54:55]\n"
  "buffer_load_dwordx4 v252, s[20:23], s46 offen nt lds\n"
  "buffer_load_dwordx4 v247, s[20:23], s47 offen offset:3456 nt lds\n"
  "s_mov_b64 exec, -1\n"
  "s_add_u32 m0, s81, 13824\n"
  "v_cndmask_b32_e64 v224, 0, -1, vcc\n"
  "v_cmp_eq_u32 vcc, 1, v251\n"
  "v_bfe_u32 v247, v241, 1, 2\n"
  "v_lshl_add_u32 v247, v250, 2, v247\n"
  "v_cndmask_b32_e64 v223, 0, -1, vcc\n"
  "v_cmp_eq_u32 vcc, 2, v251\n"
  "v_and_b32 v222, 1, v241\n"
  "v_lshlrev_b32 v222, 4, v222\n"
  "v_cndmask_b32_e64 v221, 0, -1, vcc\n"
  "s_mov_b64 exec, s[54:55]\n"
  "buffer_load_dwordx4 v246, s[20:23], s48 offen nt lds\n"
  "buffer_load_dwordx4 v245, s[20:23], s49 offen offset:3456 nt lds\n"
  "s_mov_b64 exec, -1\n"
  "s_mov_b32 s28, s8\n"
  "s_and_b32 s29, s9, 0xffff\n"
  "s_mov_b32 s30, 221184\n"
  "s_mov_b32 s31, 0x20000\n"
  "s_mov_b32 s32, s10\n"
  "s_and_b32 s33, s11, 0xffff\n"
  "s_mov_b32 s34, 73728\n"
  "s_mov_b32 s35, 0x20000\n"
  "s_cmp_eq_u32 s40, 1\n"
  "s_cselect_b64 s[36:37], s[32:33], s[28:29]\n"
  "s_cselect_b32 s38, s34, s30\n"
  "s_cmp_ge_u32 s40, 2\n"
  "s_cselect_b32 s38, 0, s38\n"
  "s_lshl_b32 s82, s90, 13\n"
  "buffer_load_dword v5, v3, s[36:39], s82 offen\n"
  "v_cmp_eq_u32 vcc, 3, v251\n"
  "v_mad_u32_u24 v246, v238, 1, 1\n"
  "v_add3_u32 v245, v246, v241, 6\n"
  "v_cndmask_b32_e64 v220, 0, -1, vcc\n"
  "v_cmp_eq_u32 vcc, 0, v247\n"
  "v_and_b32 v245, 7, v245\n"
  "v_and_b32 v252, 3, v241\n"
  "v_cndmask_b32_e64 v219, 0, -1, vcc\n"
  "v_cmp_eq_u32 vcc, 1, v247\n"
  "v_mul_u32_u24 v9, 21845, v252\n"
  "s_mov_b32 s36, %8\n"
  "s_and_b32 s37, %9, 0xffff\n"
  "s_movk_i32 s38, 1024\n"
  "s_mov_b32 m0, 88960\n"
  "s_nop 0\n"
  "buffer_load_dwordx4 v236, s[36:39], 0 offen lds\n"
  "s_add_u32 m0, s81, 20736\n"
  "v_cndmask_b32_e64 v218, 0, -1, vcc\n"
  "v_cmp_eq_u32 vcc, 2, v247\n"
  "v_mad_u32_u24 v217, v238, 1, 10\n"
  "v_add3_u32 v216, v217, v241, 6\n"
  "v_cndmask_b32_e64 v215, 0, -1, vcc\n"
  "s_mov_b64 exec, s[54:55]\n"
  "buffer_load_dwordx4 v244, s[20:23], s50 offen nt lds\n"
  "buffer_load_dwordx4 v243, s[20:23], s51 offen offset:3456 nt lds\n"
  "s_mov_b64 exec, -1\n"
  "s_add_u32 m0, s81, 27648\n"
  "v_cmp_eq_u32 vcc, 3, v247\n"
  "v_and_b32 v216, 7, v216\n"
  "v_add_u32 v252, 62336, v236\n"
  "v_cndmask_b32_e64 v214, 0, -1, vcc\n"
  "v_lshl_add_u32 v244, v238, 9, v253\n"
  "v_lshl_add_u32 v244, v239, 8, v244\n"
  "v_xor_b32 v243, v231, v226\n"
  "v_lshlrev_b32 v243, 4, v243\n"
  "v_mad_u32_u24 v243, v226, s42, v243\n"
  "s_mov_b64 exec, s[54:55]\n"
  "buffer_load_dwordx4 v242, s[20:23], s52 offen nt lds\n"
  "s_mov_b64 exec, -1\n"
  "v_add_u32 v255, 2688, v255\n"
  "v_add_u32 v250, 64, v254\n"
  "v_bitop3_b32 v247, v255, v250, s91 bitop3:0x78\n"
  "v_add_u32 v242, 80, v254\n"
  "v_bitop3_b32 v236, v255, v242, s91 bitop3:0x78\n"
  "v_add_u32 v250, 96, v254\n"
  "v_bitop3_b32 v213, v255, v250, s91 bitop3:0x78\n"
  "v_add_u32 v242, 112, v254\n"
  "v_bitop3_b32 v212, v255, v242, s91 bitop3:0x78\n"
  "v_add_u32 v250, 0, v254\n"
  "v_bitop3_b32 v211, v255, v250, s91 bitop3:0x78\n"
  "v_add_u32 v242, 16, v254\n"
  "v_bitop3_b32 v210, v255, v242, s91 bitop3:0x78\n"
  "s_add_u32 m0, s81, 31232\n"
  "v_lshl_add_u32 v250, v249, 2, v251\n"
  "s_mov_b64 exec, s[54:55]\n"
  "buffer_load_dwordx4 v247, s[20:23], s44 offen nt lds\n"
  "buffer_load_dwordx4 v236, s[20:23], s45 offen offset:3456 nt lds\n"
  "s_mov_b64 exec, -1\n"
  "s_add_u32 m0, s81, 38144\n"
  "v_and_b32 v249, 3, v2\n"
  "v_lshlrev_b32 v249, 3, v249\n"
  "v_bfe_u32 v242, v250, 1, 2\n"
  "v_lshl_add_u32 v236, v250, 7, v249\n"
  "v_add_u32 v251, s81, v252\n"
  "v_mov_b32 v8, 0xff800000\n"
  "s_mov_b64 exec, 0xffff\n"
  "v_mov_b32 v8, 0\n"
  "s_mov_b64 exec, -1\n"
  "s_mov_b64 exec, s[54:55]\n"
  "buffer_load_dwordx4 v213, s[20:23], s46 offen nt lds\n"
  "buffer_load_dwordx4 v212, s[20:23], s47 offen offset:3456 nt lds\n"
  "s_mov_b64 exec, -1\n"
  "s_add_u32 m0, s81, 45056\n"
  "s_lshr_b32 s84, s40, 1\n"
  "s_and_b32 s86, s40, 1\n"
  "s_lshl_b32 s86, s86, 3\n"
  "s_lshl_b32 s87, s84, 6\n"
  "s_add_u32 s87, s87, s86\n"
  "s_mul_i32 s76, s87, 1152\n"
  "s_lshl_b32 s87, s84, 4\n"
  "s_add_u32 s87, s87, s86\n"
  "s_mul_i32 s77, s87, 1152\n"
  "s_mul_i32 s78, s87, 384\n"
  "s_mul_i32 s87, s84, 48\n"
  "s_add_u32 s87, s87, s86\n"
  "s_mul_i32 s79, s87, 384\n"
  "s_add_u32 s80, s81, 89984\n"
  "s_mov_b64 exec, s[54:55]\n"
  "buffer_load_dwordx4 v211, s[20:23], s48 offen nt lds\n"
  "buffer_load_dwordx4 v210, s[20:23], s49 offen offset:3456 nt lds\n"
  "s_mov_b64 exec, -1\n"
  "s_mov_b32 s24, s12\n"
  "s_and_b32 s25, s13, 0xffff\n"
  "s_mov_b32 s26, 19267584\n"
  "s_mov_b32 s27, 0x20000\n"
  "s_mov_b32 s43, 925353388\n"
  "s_mov_b32 s56, 4294901758\n"
  "s_mov_b32 s57, 4294901758\n"
  "s_mov_b32 s70, 4294901760\n"
  "s_mov_b32 s71, 0\n"
  "s_mov_b32 s58, 4293853166\n"
  "s_mov_b32 s59, 4293853166\n"
  "s_mov_b32 s72, 0\n"
  "s_mov_b32 s73, 65535\n"
  "s_mov_b32 s60, 4277075694\n"
  "s_mov_b32 s61, 4277075694\n"
  "s_mov_b32 s74, 0\n"
  "s_mov_b32 s75, 4294901760\n"
  "s_mov_b32 s62, 65537\n"
  "s_mov_b32 s63, 65537\n"
  "s_mov_b32 s64, 1114129\n"
  "s_mov_b32 s65, 1114129\n"
  "s_mov_b32 s66, 17891601\n"
  "s_mov_b32 s67, 17891601\n"
  "s_mov_b32 s68, 286331153\n"
  "s_mov_b32 s69, 286331153\n"
  "s_lshr_b32 s85, s40, 1\n"
  "s_lshl_b32 s85, s85, 10\n"
  "s_and_b32 s86, s40, 1\n"
  "s_lshl_b32 s86, s86, 3\n"
  "s_add_u32 s85, s85, s86\n"
  "s_add_u32 s85, s85, 4096\n"
  "s_lshl_b32 s84, s40, 2\n"
  "s_lshl_b32 s83, s40, 1\n"
  "s_add_u32 s83, s83, 89472\n"
  "s_lshl_b32 s82, s40, 1\n"
  "v_add_u32 v247, s82, v239\n"
  "v_xor_b32 v237, v237, v247\n"
  "v_lshlrev_b32 v237, 4, v237\n"
  "v_mad_u32_u24 v250, v248, s42, v237\n"
  "v_xor_b32 v245, v245, v247\n"
  "v_lshlrev_b32 v245, 4, v245\n"
  "v_mad_u32_u24 v249, v246, s42, v245\n"
  "v_xor_b32 v216, v216, v247\n"
  "v_lshlrev_b32 v216, 4, v216\n"
  "v_mad_u32_u24 v248, v217, s42, v216\n"
  "v_add_u32 v247, s85, v252\n"
  "v_add_u32 v246, s84, v244\n"
  "s_mul_i32 s84, s40, 3072\n"
  "s_add_u32 s84, s84, 114560\n"
  "v_add_u32 v245, s84, v243\n"
  "s_mul_i32 s84, s40, 3\n"
  "s_add_u32 s84, s84, 0\n"
  "s_and_b32 s85, s84, 3\n"
  "s_lshr_b32 s84, s84, 2\n"
  "s_lshl_b32 s84, s84, 13\n"
  "s_add_u32 s86, s84, 89984\n"
  "v_xor_b32 v244, s85, v242\n"
  "v_lshl_add_u32 v244, v244, 5, v236\n"
  "v_add_u32 v237, s86, v244\n"
  "s_mul_i32 s84, s40, 3\n"
  "s_add_u32 s84, s84, 1\n"
  "s_and_b32 s85, s84, 3\n"
  "s_lshr_b32 s84, s84, 2\n"
  "s_lshl_b32 s84, s84, 13\n"
  "s_add_u32 s86, s84, 89984\n"
  "v_xor_b32 v243, s85, v242\n"
  "v_lshl_add_u32 v243, v243, 5, v236\n"
  "v_add_u32 v198, s86, v243\n"
  "s_mul_i32 s84, s40, 3\n"
  "s_add_u32 s84, s84, 2\n"
  "s_and_b32 s85, s84, 3\n"
  "s_lshr_b32 s84, s84, 2\n"
  "s_lshl_b32 s84, s84, 13\n"
  "s_add_u32 s86, s84, 89984\n"
  "v_xor_b32 v244, s85, v242\n"
  "v_lshl_add_u32 v244, v244, 5, v236\n"
  "v_add_u32 v197, s86, v244\n"
  "s_mov_b32 s85, s40\n"
  "s_mov_b32 s86, 89984\n"
  "v_xor_b32 v243, s85, v242\n"
  "v_lshl_add_u32 v243, v243, 5, v236\n"
  "v_add_u32 v15, s86, v243\n"
  "v_mov_b32 v11, 0\n"
  "v_mad_u32_u24 v236, v226, s42, v229\n"
  "v_mov_b32 v16, 0x3f803f80\n"
  "v_mov_b32 v17, 0x3f803f80\n"
  "v_mov_b32 v18, 0x3f803f80\n"
  "v_mov_b32 v19, 0x3f803f80\n"
  "v_and_b32 v244, 7, v241\n"
  "v_add_u32 v242, s82, v238\n"
  "v_xor_b32 v242, v242, v244\n"
  "v_lshlrev_b32 v242, 4, v242\n"
  "v_mad_u32_u24 v242, v241, s42, v242\n"
  "v_lshl_add_u32 v242, v239, 3, v242\n"
  "v_add_u32 v244, 114560, v242\n"
  "v_lshlrev_b32 v241, 6, v226\n"
  "v_add_u32 v243, s83, v241\n"
  "v_lshlrev_b32 v242, 4, v231\n"
  "v_mov_b32 v12, 0\n"
  "v_mov_b32 v13, 0\n"
  "s_waitcnt vmcnt(25)\n"
  "v_and_b32 v20, v224, v235\n"
  "v_and_b32 v21, v223, v235\n"
  "v_and_b32 v22, v221, v235\n"
  "s_waitcnt vmcnt(23)\n"
  "v_lshlrev_b32 v233, v222, v233\n"
  "v_and_b32 v24, v219, v233\n"
  "v_and_b32 v25, v218, v233\n"
  "v_and_b32 v26, v215, v233\n"
  "v_and_b32 v27, v214, v233\n"
  "v_and_b32 v23, v220, v235\n"
  "s_mov_b64 exec, 0xffff\n"
  "v_mov_b32 v234, 0x3f80\n"
  "s_mov_b64 exec, -1\n"
  "v_and_b32 v28, v224, v234\n"
  "v_and_b32 v29, v223, v234\n"
  "v_and_b32 v30, v221, v234\n"
  "v_and_b32 v31, v220, v234\n"
  "s_waitcnt vmcnt(22)\n"
  "v_and_b32 v32, v224, v232\n"
  "v_and_b32 v33, v223, v232\n"
  "v_and_b32 v34, v221, v232\n"
  "s_waitcnt vmcnt(20)\n"
  "v_lshlrev_b32 v228, v222, v228\n"
  "v_and_b32 v200, v219, v228\n"
  "v_and_b32 v201, v218, v228\n"
  "v_and_b32 v202, v215, v228\n"
  "v_and_b32 v203, v214, v228\n"
  "v_and_b32 v35, v220, v232\n"
  "s_mov_b64 exec, 0xffff\n"
  "v_mov_b32 v230, 0x3f80\n"
  "s_mov_b64 exec, -1\n"
  "v_and_b32 v36, v224, v230\n"
  "v_and_b32 v37, v223, v230\n"
  "v_and_b32 v38, v221, v230\n"
  "v_and_b32 v39, v220, v230\n"
  "s_waitcnt vmcnt(19)\n"
  "v_and_b32 v40, v224, v227\n"
  "v_and_b32 v41, v223, v227\n"
  "v_and_b32 v42, v221, v227\n"
  "s_waitcnt vmcnt(17)\n"
  "v_lshlrev_b32 v225, v222, v225\n"
  "v_and_b32 v44, v219, v225\n"
  "v_and_b32 v45, v218, v225\n"
  "v_and_b32 v46, v215, v225\n"
  "v_and_b32 v47, v214, v225\n"
  "v_and_b32 v43, v220, v227\n"
  "s_mov_b64 exec, 0xffff\n"
  "v_mov_b32 v240, 0x3f80\n"
  "s_mov_b64 exec, -1\n"
  "v_and_b32 v48, v224, v240\n"
  "v_and_b32 v49, v223, v240\n"
  "v_and_b32 v50, v221, v240\n"
  "v_and_b32 v51, v220, v240\n"
  "s_waitcnt vmcnt(10)\n"
  "s_nop 0\n"
  "s_waitcnt vmcnt(9)\n"
  "ds_read_u16 v241, v253 offset:2048\n"
  "ds_read_u16 v240, v253 offset:2050\n"
  "ds_read_u16 v239, v253 offset:2052\n"
  "ds_read_u16 v238, v253 offset:2054\n"
  "s_waitcnt lgkmcnt(3)\n"
  "v_add_u32 v235, v241, v250\n"
  "v_add_u32 v234, v241, v249\n"
  "v_add_u32 v233, v241, v248\n"
  "s_waitcnt lgkmcnt(2)\n"
  "v_add_u32 v232, v240, v250\n"
  "v_add_u32 v241, v240, v249\n"
  "v_add_u32 v231, v240, v248\n"
  "s_waitcnt lgkmcnt(1)\n"
  "v_add_u32 v230, v239, v250\n"
  "v_add_u32 v240, v239, v249\n"
  "v_add_u32 v229, v239, v248\n"
  "s_waitcnt lgkmcnt(0)\n"
  "v_add_u32 v228, v238, v250\n"
  "v_add_u32 v239, v238, v249\n"
  "v_add_u32 v196, v238, v248\n"
  "s_waitcnt vmcnt(6)\n"
  "s_barrier\n"
  "ds_read_b128 v[52:55], v235 offset:0\n"
  "ds_read_b128 v[56:59], v234 offset:0\n"
  "ds_read_b128 v[60:63], v234 offset:3072\n"
  "s_add_u32 m0, s80, 0\n"
  "s_add_u32 s87, s76, 384\n"
  "buffer_load_dwordx4 v199, s[28:31], s87 offen lds\n"
  "ds_read_b128 v[64:67], v233 offset:0\n"
  "ds_read_b128 v[68:71], v233 offset:3072\n"
  "s_add_u32 m0, s80, 4096\n"
  "s_add_u32 s87, s76, 18816\n"
  "buffer_load_dwordx4 v199, s[28:31], s87 offen lds\n"
  "ds_read_b128 v[72:75], v235 offset:128\n"
  "ds_read_b128 v[76:79], v234 offset:128\n"
  "s_waitcnt lgkmcnt(6)\n"
  "v_mfma_f32_16x16x32_bf16 v[204:207], v[24:27], v[52:55], 0\n"
  "ds_read_b128 v[80:83], v234 offset:3200\n"
  "ds_read_b128 v[84:87], v233 offset:128\n"
  "s_add_u32 m0, s80, 8192\n"
  "s_add_u32 s87, s76, 512\n"
  "buffer_load_dwordx4 v199, s[28:31], s87 offen lds\n"
  "s_waitcnt lgkmcnt(6)\n"
  "v_smfmac_f32_16x16x64_bf16 v[204:207], v[20:23], v[56:63], v9\n"
  "s_waitcnt lgkmcnt(5)\n"
  "ds_read_b128 v[88:91], v233 offset:3200\n"
  "ds_read_b128 v[52:55], v235 offset:256\n"
  "s_waitcnt lgkmcnt(6)\n"
  "v_smfmac_f32_16x16x64_bf16 v[204:207], v[28:31], v[64:71], v9\n"
  "ds_read_b128 v[56:59], v234 offset:256\n"
  "s_nop 0\n"
  "s_waitcnt lgkmcnt(6)\n"
  "v_mfma_f32_16x16x32_bf16 v[208:211], v[200:203], v[72:75], 0\n"
  "s_waitcnt lgkmcnt(5)\n"
  "ds_read_b128 v[60:63], v234 offset:3328\n"
  "s_add_u32 m0, s80, 12288\n"
  "s_add_u32 s87, s76, 18944\n"
  "buffer_load_dwordx4 v199, s[28:31], s87 offen lds\n"
  "s_waitcnt lgkmcnt(5)\n"
  "v_smfmac_f32_16x16x64_bf16 v[208:211], v[32:35], v[76:83], v9\n"
  "s_waitcnt lgkmcnt(4)\n"
  "ds_read_b128 v[64:67], v233 offset:256\n"
  "ds_read_b128 v[68:71], v233 offset:3328\n"
  "ds_read_b128 v[72:75], v232 offset:0\n"
  "v_add_f32 v238, v204, v205\n"
  "s_waitcnt lgkmcnt(6)\n"
  "v_smfmac_f32_16x16x64_bf16 v[208:211], v[36:39], v[84:91], v9\n"
  "ds_read_b128 v[76:79], v241 offset:0\n"
  "s_add_u32 m0, s80, 16384\n"
  "s_add_u32 s87, s76, 640\n"
  "buffer_load_dwordx4 v199, s[28:31], s87 offen lds\n"
  "s_waitcnt lgkmcnt(6)\n"
  "v_mfma_f32_16x16x32_bf16 v[212:215], v[44:47], v[52:55], 0\n"
  "s_waitcnt lgkmcnt(5)\n"
  "ds_read_b128 v[80:83], v241 offset:3072\n"
  "ds_read_b128 v[84:87], v231 offset:0\n"
  "s_waitcnt lgkmcnt(6)\n"
  "v_smfmac_f32_16x16x64_bf16 v[212:215], v[40:43], v[56:63], v9\n"
  "s_waitcnt lgkmcnt(5)\n"
  "ds_read_b128 v[88:91], v231 offset:3072\n"
  "v_mul_f32 v235, v204, v204\n"
  "v_fmac_f32 v235, v205, v205\n"
  "v_add_f32 v238, v206, v238\n"
  "s_waitcnt lgkmcnt(5)\n"
  "v_smfmac_f32_16x16x64_bf16 v[212:215], v[48:51], v[64:71], v9\n"
  "ds_read_b128 v[52:55], v232 offset:128\n"
  "ds_read_b128 v[56:59], v241 offset:128\n"
  "s_waitcnt lgkmcnt(6)\n"
  "v_mfma_f32_16x16x32_bf16 v[216:219], v[24:27], v[72:75], 0\n"
  "s_waitcnt lgkmcnt(5)\n"
  "ds_read_b128 v[60:63], v241 offset:3200\n"
  "s_add_u32 m0, s80, 20480\n"
  "s_add_u32 s87, s76, 19072\n"
  "buffer_load_dwordx4 v199, s[28:31], s87 offen lds\n"
  "s_waitcnt lgkmcnt(5)\n"
  "v_smfmac_f32_16x16x64_bf16 v[216:219], v[20:23], v[76:83], v9\n"
  "s_waitcnt lgkmcnt(4)\n"
  "ds_read_b128 v[64:67], v231 offset:128\n"
  "ds_read_b128 v[68:71], v231 offset:3200\n"
  "ds_read_b128 v[72:75], v232 offset:256\n"
  "s_add_u32 m0, s80, 24576\n"
  "s_add_u32 s87, s76, 37248\n"
  "buffer_load_dwordx4 v199, s[28:31], s87 offen lds\n"
  "s_waitcnt lgkmcnt(6)\n"
  "v_smfmac_f32_16x16x64_bf16 v[216:219], v[28:31], v[84:91], v9\n"
  "s_waitcnt lgkmcnt(5)\n"
  "ds_read_b128 v[76:79], v241 offset:256\n"
  "ds_read_b128 v[80:83], v241 offset:3328\n"
  "v_mfma_f32_16x16x32_bf16 v[220:223], v[200:203], v[52:55], 0\n"
  "s_waitcnt lgkmcnt(6)\n"
  "ds_read_b128 v[84:87], v231 offset:256\n"
  "v_fmac_f32 v235, v206, v206\n"
  "s_waitcnt lgkmcnt(6)\n"
  "v_smfmac_f32_16x16x64_bf16 v[220:223], v[32:35], v[56:63], v9\n"
  "ds_read_b128 v[88:91], v231 offset:3328\n"
  "s_add_u32 m0, s80, 28672\n"
  "s_add_u32 s87, s76, 55680\n"
  "buffer_load_dwordx4 v199, s[28:31], s87 offen lds\n"
  "s_waitcnt lgkmcnt(5)\n"
  "v_smfmac_f32_16x16x64_bf16 v[220:223], v[36:39], v[64:71], v9\n"
  "ds_read_b128 v[56:59], v230 offset:0\n"
  "ds_read_b128 v[92:95], v240 offset:0\n"
  "s_waitcnt lgkmcnt(6)\n"
  "v_mfma_f32_16x16x32_bf16 v[224:227], v[44:47], v[72:75], 0\n"
  "s_waitcnt lgkmcnt(5)\n"
  "ds_read_b128 v[96:99], v240 offset:3072\n"
  "s_add_u32 m0, s80, 32768\n"
  "s_add_u32 s87, s76, 37376\n"
  "buffer_load_dwordx4 v199, s[28:31], s87 offen lds\n"
  "s_waitcnt lgkmcnt(5)\n"
  "v_smfmac_f32_16x16x64_bf16 v[224:227], v[40:43], v[76:83], v9\n"
  "s_waitcnt lgkmcnt(4)\n"
  "ds_read_b128 v[60:63], v229 offset:0\n"
  "ds_read_b128 v[64:67], v229 offset:3072\n"
  "ds_read_b128 v[68:71], v230 offset:128\n"
  "v_add_f32 v238, v207, v238\n"
  "s_waitcnt lgkmcnt(6)\n"
  "v_smfmac_f32_16x16x64_bf16 v[224:227], v[48:51], v[84:91], v9\n"
  "ds_read_b128 v[72:75], v240 offset:128\n"
  "v_fmac_f32 v235, v207, v207\n"
  "s_waitcnt lgkmcnt(6)\n"
  "v_mfma_f32_16x16x32_bf16 v[52:55], v[24:27], v[56:59], 0\n"
  "s_waitcnt lgkmcnt(5)\n"
  "ds_read_b128 v[76:79], v240 offset:3200\n"
  "s_add_u32 m0, s80, 36864\n"
  "s_add_u32 s87, s76, 55808\n"
  "buffer_load_dwordx4 v199, s[28:31], s87 offen lds\n"
  "s_waitcnt lgkmcnt(5)\n"
  "v_smfmac_f32_16x16x64_bf16 v[52:55], v[20:23], v[92:99], v9\n"
  "s_waitcnt lgkmcnt(4)\n"
  "ds_read_b128 v[80:83], v229 offset:128\n"
  "ds_read_b128 v[84:87], v229 offset:3200\n"
  "ds_read_b128 v[88:91], v230 offset:256\n"
  "v_add_f32 v238, v208, v238\n"
  "s_waitcnt lgkmcnt(6)\n"
  "v_smfmac_f32_16x16x64_bf16 v[52:55], v[28:31], v[60:67], v9\n"
  "ds_read_b128 v[92:95], v240 offset:256\n"
  "s_add_u32 m0, s80, 40960\n"
  "s_add_u32 s87, s76, 37504\n"
  "buffer_load_dwordx4 v199, s[28:31], s87 offen lds\n"
  "s_waitcnt lgkmcnt(6)\n"
  "v_mfma_f32_16x16x32_bf16 v[56:59], v[200:203], v[68:71], 0\n"
  "s_waitcnt lgkmcnt(5)\n"
  "ds_read_b128 v[96:99], v240 offset:3328\n"
  "ds_read_b128 v[64:67], v229 offset:256\n"
  "s_waitcnt lgkmcnt(6)\n"
  "v_smfmac_f32_16x16x64_bf16 v[56:59], v[32:35], v[72:79], v9\n"
  "s_waitcnt lgkmcnt(5)\n"
  "ds_read_b128 v[68:71], v229 offset:3328\n"
  "s_add_u32 m0, s80, 45056\n"
  "s_add_u32 s87, s76, 55936\n"
  "buffer_load_dwordx4 v199, s[28:31], s87 offen lds\n"
  "s_waitcnt lgkmcnt(5)\n"
  "v_smfmac_f32_16x16x64_bf16 v[56:59], v[36:39], v[80:87], v9\n"
  "s_waitcnt lgkmcnt(4)\n"
  "s_waitcnt vmcnt(12)\n"
  "s_barrier\n"
  "v_mfma_f32_16x16x32_bf16 v[60:63], v[44:47], v[88:91], 0\n"
  "s_waitcnt lgkmcnt(3)\n"
  "ds_read_b128 v[72:75], v228 offset:0\n"
  "ds_read_b128 v[76:79], v239 offset:0\n"
  "s_waitcnt lgkmcnt(4)\n"
  "v_smfmac_f32_16x16x64_bf16 v[60:63], v[40:43], v[92:99], v9\n"
  "s_waitcnt lgkmcnt(3)\n"
  "ds_read_b128 v[80:83], v239 offset:3072\n"
  "ds_read_b128 v[84:87], v196 offset:0\n"
  "s_add_u32 m0, s80, 49152\n"
  "s_add_u32 s87, s77, 147840\n"
  "buffer_load_dwordx4 v199, s[28:31], s87 offen lds\n"
  "s_waitcnt lgkmcnt(4)\n"
  "v_smfmac_f32_16x16x64_bf16 v[60:63], v[48:51], v[64:71], v9\n"
  "ds_read_b128 v[88:91], v196 offset:3072\n"
  "ds_read_b128 v[92:95], v228 offset:128\n"
  "ds_read_b128 v[96:99], v239 offset:128\n"
  "s_add_u32 m0, s80, 53248\n"
  "s_add_u32 s87, s77, 184704\n"
  "buffer_load_dwordx4 v199, s[28:31], s87 offen lds\n"
  "s_waitcnt lgkmcnt(6)\n"
  "v_mfma_f32_16x16x32_bf16 v[64:67], v[24:27], v[72:75], 0\n"
  "s_waitcnt lgkmcnt(5)\n"
  "ds_read_b128 v[100:103], v239 offset:3200\n"
  "ds_read_b128 v[104:107], v196 offset:128\n"
  "s_waitcnt lgkmcnt(6)\n"
  "v_smfmac_f32_16x16x64_bf16 v[64:67], v[20:23], v[76:83], v9\n"
  "ds_read_b128 v[108:111], v196 offset:3200\n"
  "ds_read_b128 v[112:115], v228 offset:256\n"
  "s_add_u32 m0, s80, 57344\n"
  "s_add_u32 s87, s77, 147968\n"
  "buffer_load_dwordx4 v199, s[28:31], s87 offen lds\n"
  "s_waitcnt lgkmcnt(6)\n"
  "v_smfmac_f32_16x16x64_bf16 v[64:67], v[28:31], v[84:91], v9\n"
  "s_waitcnt lgkmcnt(5)\n"
  "ds_read_b128 v[76:79], v239 offset:256\n"
  "ds_read_b128 v[80:83], v239 offset:3328\n"
  "v_mfma_f32_16x16x32_bf16 v[68:71], v[200:203], v[92:95], 0\n"
  "s_waitcnt lgkmcnt(6)\n"
  "ds_read_b128 v[84:87], v196 offset:256\n"
  "v_fmac_f32 v235, v208, v208\n"
  "s_waitcnt lgkmcnt(6)\n"
  "v_smfmac_f32_16x16x64_bf16 v[68:71], v[32:35], v[96:103], v9\n"
  "ds_read_b128 v[88:91], v196 offset:3328\n"
  "s_add_u32 m0, s80, 61440\n"
  "s_add_u32 s87, s77, 184832\n"
  "buffer_load_dwordx4 v199, s[28:31], s87 offen lds\n"
  "s_waitcnt lgkmcnt(5)\n"
  "v_smfmac_f32_16x16x64_bf16 v[68:71], v[36:39], v[104:111], v9\n"
  "s_waitcnt vmcnt(10)\n"
  "s_barrier\n"
  "s_waitcnt lgkmcnt(4)\n"
  "v_mfma_f32_16x16x32_bf16 v[72:75], v[44:47], v[112:115], 0\n"
  "s_waitcnt lgkmcnt(3)\n"
  "ds_read_b64_tr_b16 a[72:73], v237 offset:0\n"
  "ds_read_b64_tr_b16 a[74:75], v237 offset:2048\n"
  "s_waitcnt lgkmcnt(4)\n"
  "v_smfmac_f32_16x16x64_bf16 v[72:75], v[40:43], v[76:83], v9\n"
  "s_waitcnt lgkmcnt(3)\n"
  "ds_read_b64_tr_b16 a[96:97], v198 offset:0\n"
  "ds_read_b64_tr_b16 a[98:99], v198 offset:2048\n"
  "ds_read_b64_tr_b16 a[120:121], v197 offset:0\n"
  "ds_read_b64_tr_b16 a[122:123], v197 offset:2048\n"
  "s_waitcnt lgkmcnt(6)\n"
  "v_smfmac_f32_16x16x64_bf16 v[72:75], v[48:51], v[84:91], v9\n"
  "ds_read_b64_tr_b16 a[76:77], v237 offset:4096\n"
  "ds_read_b64_tr_b16 a[78:79], v237 offset:6144\n"
  "ds_read_b64_tr_b16 a[100:101], v198 offset:4096\n"
  "ds_read_b64_tr_b16 a[102:103], v198 offset:6144\n"
  "ds_read_b64_tr_b16 a[124:125], v197 offset:4096\n"
  "ds_read_b64_tr_b16 a[126:127], v197 offset:6144\n"
  "s_add_u32 m0, s80, 65536\n"
  "s_add_u32 s87, s77, 148096\n"
  "buffer_load_dwordx4 v199, s[28:31], s87 offen lds\n"
  "v_add_f32 v238, v209, v238\n"
  "v_fmac_f32 v235, v209, v209\n"
  "v_add_f32 v238, v210, v238\n"
  "v_fmac_f32 v235, v210, v210\n"
  "v_add_f32 v238, v211, v238\n"
  "v_fmac_f32 v235, v211, v211\n"
  "v_add_f32 v238, v212, v238\n"
  "v_fmac_f32 v235, v212, v212\n"
  "v_add_f32 v238, v213, v238\n"
  "v_fmac_f32 v235, v213, v213\n"
  "v_add_f32 v238, v214, v238\n"
  "v_fmac_f32 v235, v214, v214\n"
  "v_add_f32 v238, v215, v238\n"
  "v_fmac_f32 v235, v215, v215\n"
  "v_add_f32 v241, v216, v217\n"
  "v_mul_f32 v240, v216, v216\n"
  "v_permlane16_swap_b32 v238, v235\n"
  "v_add_f32 v238, v238, v235\n"
  "v_fmac_f32 v240, v217, v217\n"
  "v_add_f32 v241, v218, v241\n"
  "v_fmac_f32 v240, v218, v218\n"
  "v_add_f32 v241, v219, v241\n"
  "v_fmac_f32 v240, v219, v219\n"
  "v_add_f32 v241, v220, v241\n"
  "v_fmac_f32 v240, v220, v220\n"
  "v_add_f32 v241, v221, v241\n"
  "v_fmac_f32 v240, v221, v221\n"
  "v_add_f32 v241, v222, v241\n"
  "v_fmac_f32 v240, v222, v222\n"
  "v_add_f32 v241, v223, v241\n"
  "v_fmac_f32 v240, v223, v223\n"
  "v_add_f32 v241, v224, v241\n"
  "v_fmac_f32 v240, v224, v224\n"
  "v_add_f32 v241, v225, v241\n"
  "v_fmac_f32 v240, v225, v225\n"
  "v_add_f32 v241, v226, v241\n"
  "v_fmac_f32 v240, v226, v226\n"
  "v_add_f32 v241, v227, v241\n"
  "v_fmac_f32 v240, v227, v227\n"
  "v_add_f32 v239, v52, v53\n"
  "v_mul_f32 v235, v52, v52\n"
  "v_permlane16_swap_b32 v241, v240\n"
  "v_add_f32 v241, v241, v240\n"
  "v_fmac_f32 v235, v53, v53\n"
  "v_add_f32 v239, v54, v239\n"
  "v_permlane32_swap_b32 v238, v241\n"
  "v_add_f32 v238, v238, v241\n"
  "ds_write_b32 v246, v238 offset:0\n"
  "s_add_u32 m0, s80, 69632\n"
  "s_add_u32 s87, s77, 184960\n"
  "buffer_load_dwordx4 v199, s[28:31], s87 offen lds\n"
  "v_fmac_f32 v235, v54, v54\n"
  "v_add_f32 v239, v55, v239\n"
  "v_fmac_f32 v235, v55, v55\n"
  "v_add_f32 v239, v56, v239\n"
  "v_fmac_f32 v235, v56, v56\n"
  "v_add_f32 v239, v57, v239\n"
  "v_fmac_f32 v235, v57, v57\n"
  "v_add_f32 v239, v58, v239\n"
  "v_fmac_f32 v235, v58, v58\n"
  "v_add_f32 v239, v59, v239\n"
  "v_fmac_f32 v235, v59, v59\n"
  "v_add_f32 v239, v60, v239\n"
  "v_fmac_f32 v235, v60, v60\n"
  "v_add_f32 v239, v61, v239\n"
  "v_fmac_f32 v235, v61, v61\n"
  "v_add_f32 v239, v62, v239\n"
  "v_fmac_f32 v235, v62, v62\n"
  "v_add_f32 v239, v63, v239\n"
  "v_fmac_f32 v235, v63, v63\n"
  "v_add_f32 v241, v64, v65\n"
  "v_mul_f32 v240, v64, v64\n"
  "v_permlane16_swap_b32 v239, v235\n"
  "v_add_f32 v239, v239, v235\n"
  "v_fmac_f32 v240, v65, v65\n"
  "v_add_f32 v241, v66, v241\n"
  "v_fmac_f32 v240, v66, v66\n"
  "v_add_f32 v241, v67, v241\n"
  "v_fmac_f32 v240, v67, v67\n"
  "v_add_f32 v241, v68, v241\n"
  "v_fmac_f32 v240, v68, v68\n"
  "v_add_f32 v241, v69, v241\n"
  "v_fmac_f32 v240, v69, v69\n"
  "v_add_f32 v241, v70, v241\n"
  "v_fmac_f32 v240, v70, v70\n"
  "v_add_f32 v241, v71, v241\n"
  "v_fmac_f32 v240, v71, v71\n"
  "v_add_f32 v241, v72, v241\n"
  "v_fmac_f32 v240, v72, v72\n"
  "v_add_f32 v241, v73, v241\n"
  "v_fmac_f32 v240, v73, v73\n"
  "v_add_f32 v241, v74, v241\n"
  "v_fmac_f32 v240, v74, v74\n"
  "v_add_f32 v241, v75, v241\n"
  "v_fmac_f32 v240, v75, v75\n"
  "s_nop 0\n"
  "s_nop 0\n"
  "v_permlane16_swap_b32 v241, v240\n"
  "v_add_f32 v241, v241, v240\n"
  "s_nop 0\n"
  "s_nop 0\n"
  "v_permlane32_swap_b32 v239, v241\n"
  "v_add_f32 v239, v239, v241\n"
  "ds_write_b32 v246, v239 offset:1024\n"
  "s_waitcnt vmcnt(6) lgkmcnt(0)\n"
  "s_barrier\n"
  "s_add_u32 m0, s80, 0\n"
  "s_add_u32 s87, s76, 0\n"
  "buffer_load_dwordx4 v199, s[28:31], s87 offen lds\n"
  "ds_read_b64_tr_b16 a[80:81], v237 offset:24576\n"
  "ds_read_b64_tr_b16 a[82:83], v237 offset:26624\n"
  "ds_read_b64_tr_b16 a[104:105], v198 offset:24576\n"
  "ds_read_b64_tr_b16 a[106:107], v198 offset:26624\n"
  "s_add_u32 m0, s80, 4096\n"
  "s_add_u32 s87, s76, 18432\n"
  "buffer_load_dwordx4 v199, s[28:31], s87 offen lds\n"
  "ds_read_b64_tr_b16 a[128:129], v197 offset:24576\n"
  "ds_read_b64_tr_b16 a[130:131], v197 offset:26624\n"
  "ds_read_b64_tr_b16 a[84:85], v237 offset:28672\n"
  "ds_read_b64_tr_b16 a[86:87], v237 offset:30720\n"
  "ds_read_b64_tr_b16 a[108:109], v198 offset:28672\n"
  "ds_read_b64_tr_b16 a[110:111], v198 offset:30720\n"
  "ds_read_b64_tr_b16 a[132:133], v197 offset:28672\n"
  "ds_read_b64_tr_b16 a[134:135], v197 offset:30720\n"
  "ds_read_b128 v[76:79], v253 offset:0\n"
  "s_add_u32 m0, s80, 8192\n"
  "s_add_u32 s87, s76, 128\n"
  "buffer_load_dwordx4 v199, s[28:31], s87 offen lds\n"
  "ds_read_b128 v[80:83], v253 offset:256\n"
  "ds_read_b128 v[84:87], v253 offset:512\n"
  "ds_read_b128 v[88:91], v253 offset:768\n"
  "ds_read_b128 v[92:95], v253 offset:1024\n"
  "s_add_u32 m0, s80, 12288\n"
  "s_add_u32 s87, s76, 18560\n"
  "buffer_load_dwordx4 v199, s[28:31], s87 offen lds\n"
  "ds_read_b128 v[96:99], v253 offset:1280\n"
  "ds_read_b128 v[100:103], v253 offset:1536\n"
  "s_waitcnt lgkmcnt(6)\n"
  "v_add_f32 v76, v76, v77\n"
  "v_add_f32 v78, v78, v79\n"
  "v_add_f32 v76, v76, v78\n"
  "s_waitcnt lgkmcnt(5)\n"
  "v_add_f32 v80, v80, v81\n"
  "s_add_u32 m0, s80, 16384\n"
  "s_add_u32 s87, s76, 256\n"
  "buffer_load_dwordx4 v199, s[28:31], s87 offen lds\n"
  "ds_read_b128 v[104:107], v253 offset:1792\n"
  "v_add_f32 v82, v82, v83\n"
  "v_add_f32 v80, v80, v82\n"
  "v_mul_f32 v76, 0xbbaaaaab, v76\n"
  "v_mul_f32 v80, 0x3baaaaab, v80\n"
  "v_fma_f32 v80, -v76, v76, v80\n"
  "v_add_f32_e64 v80, abs(v80), s43\n"
  "v_rsq_f32 v80, v80\n"
  "s_add_u32 m0, s80, 20480\n"
  "s_add_u32 s87, s76, 18688\n"
  "buffer_load_dwordx4 v199, s[28:31], s87 offen lds\n"
  "v_mul_f32 v76, v76, v80\n"
  "v_fma_f32 v77, v204, v80, v76\n"
  "v_fma_f32 v78, v205, v80, v76\n"
  "v_cvt_pk_bf16_f32 v108, v77, v78\n"
  "v_fma_f32 v81, v206, v80, v76\n"
  "v_fma_f32 v82, v207, v80, v76\n"
  "v_cvt_pk_bf16_f32 v109, v81, v82\n"
  "v_fma_f32 v77, v208, v80, v76\n"
  "v_fma_f32 v78, v209, v80, v76\n"
  "v_cvt_pk_bf16_f32 v110, v77, v78\n"
  "v_fma_f32 v81, v210, v80, v76\n"
  "v_fma_f32 v82, v211, v80, v76\n"
  "v_cvt_pk_bf16_f32 v111, v81, v82\n"
  "ds_write_b128 v251, v[108:111] offset:0\n"
  "v_fma_f32 v77, v212, v80, v76\n"
  "v_fma_f32 v78, v213, v80, v76\n"
  "v_cvt_pk_bf16_f32 v240, v77, v78\n"
  "v_fma_f32 v81, v214, v80, v76\n"
  "v_fma_f32 v82, v215, v80, v76\n"
  "v_cvt_pk_bf16_f32 v241, v81, v82\n"
  "ds_write_b64 v247, v[240:241] offset:0\n"
  "s_waitcnt vmcnt(6)\n"
  "s_barrier\n"
  "s_add_u32 m0, s80, 24576\n"
  "s_add_u32 s87, s76, 36864\n"
  "buffer_load_dwordx4 v199, s[28:31], s87 offen lds\n"
  "ds_read_b64_tr_b16 a[88:89], v237 offset:49152\n"
  "ds_read_b64_tr_b16 a[90:91], v237 offset:51200\n"
  "ds_read_b64_tr_b16 a[112:113], v198 offset:49152\n"
  "ds_read_b64_tr_b16 a[114:115], v198 offset:51200\n"
  "s_add_u32 m0, s80, 28672\n"
  "s_add_u32 s87, s76, 55296\n"
  "buffer_load_dwordx4 v199, s[28:31], s87 offen lds\n"
  "ds_read_b64_tr_b16 a[136:137], v197 offset:49152\n"
  "ds_read_b64_tr_b16 a[138:139], v197 offset:51200\n"
  "ds_read_b64_tr_b16 a[92:93], v237 offset:53248\n"
  "ds_read_b64_tr_b16 a[94:95], v237 offset:55296\n"
  "ds_read_b64_tr_b16 a[116:117], v198 offset:53248\n"
  "ds_read_b64_tr_b16 a[118:119], v198 offset:55296\n"
  "ds_read_b64_tr_b16 a[140:141], v197 offset:53248\n"
  "ds_read_b64_tr_b16 a[142:143], v197 offset:55296\n"
  "s_add_u32 m0, s80, 32768\n"
  "s_add_u32 s87, s76, 36992\n"
  "buffer_load_dwordx4 v199, s[28:31], s87 offen lds\n"
  "s_waitcnt lgkmcnt(15)\n"
  "v_add_f32 v84, v84, v85\n"
  "v_add_f32 v86, v86, v87\n"
  "v_add_f32 v84, v84, v86\n"
  "v_add_f32 v88, v88, v89\n"
  "v_add_f32 v90, v90, v91\n"
  "v_add_f32 v88, v88, v90\n"
  "v_mul_f32 v84, 0xbbaaaaab, v84\n"
  "v_mul_f32 v88, 0x3baaaaab, v88\n"
  "s_add_u32 m0, s80, 36864\n"
  "s_add_u32 s87, s76, 55424\n"
  "buffer_load_dwordx4 v199, s[28:31], s87 offen lds\n"
  "v_fma_f32 v88, -v84, v84, v88\n"
  "v_add_f32_e64 v88, abs(v88), s43\n"
  "v_rsq_f32 v88, v88\n"
  "v_add_f32 v92, v92, v93\n"
  "v_mul_f32 v84, v84, v88\n"
  "v_fma_f32 v85, v216, v88, v84\n"
  "v_fma_f32 v86, v217, v88, v84\n"
  "v_cvt_pk_bf16_f32 v76, v85, v86\n"
  "s_add_u32 m0, s80, 40960\n"
  "s_add_u32 s87, s76, 37120\n"
  "buffer_load_dwordx4 v199, s[28:31], s87 offen lds\n"
  "v_fma_f32 v89, v218, v88, v84\n"
  "v_fma_f32 v90, v219, v88, v84\n"
  "v_cvt_pk_bf16_f32 v77, v89, v90\n"
  "v_fma_f32 v85, v220, v88, v84\n"
  "v_fma_f32 v86, v221, v88, v84\n"
  "v_cvt_pk_bf16_f32 v78, v85, v86\n"
  "v_fma_f32 v89, v222, v88, v84\n"
  "v_fma_f32 v90, v223, v88, v84\n"
  "v_cvt_pk_bf16_f32 v79, v89, v90\n"
  "ds_write_b128 v251, v[76:79] offset:6144\n"
  "s_add_u32 m0, s80, 45056\n"
  "s_add_u32 s87, s76, 55552\n"
  "buffer_load_dwordx4 v199, s[28:31], s87 offen lds\n"
  "v_fma_f32 v85, v224, v88, v84\n"
  "v_fma_f32 v86, v225, v88, v84\n"
  "v_cvt_pk_bf16_f32 v240, v85, v86\n"
  "v_fma_f32 v89, v226, v88, v84\n"
  "v_fma_f32 v90, v227, v88, v84\n"
  "v_cvt_pk_bf16_f32 v241, v89, v90\n"
  "ds_write_b64 v247, v[240:241] offset:6144\n"
  "v_add_f32 v94, v94, v95\n"
  "v_add_f32 v92, v92, v94\n"
  "v_add_f32 v96, v96, v97\n"
  "v_add_f32 v98, v98, v99\n"
  "v_add_f32 v96, v96, v98\n"
  "v_mul_f32 v92, 0xbbaaaaab, v92\n"
  "v_mul_f32 v96, 0x3baaaaab, v96\n"
  "v_fma_f32 v96, -v92, v92, v96\n"
  "v_add_f32_e64 v96, abs(v96), s43\n"
  "v_rsq_f32 v96, v96\n"
  "v_add_f32 v100, v100, v101\n"
  "v_mul_f32 v92, v92, v96\n"
  "v_fma_f32 v93, v52, v96, v92\n"
  "v_fma_f32 v94, v53, v96, v92\n"
  "v_cvt_pk_bf16_f32 v76, v93, v94\n"
  "v_fma_f32 v97, v54, v96, v92\n"
  "v_fma_f32 v98, v55, v96, v92\n"
  "v_cvt_pk_bf16_f32 v77, v97, v98\n"
  "v_fma_f32 v93, v56, v96, v92\n"
  "v_fma_f32 v94, v57, v96, v92\n"
  "v_cvt_pk_bf16_f32 v78, v93, v94\n"
  "v_fma_f32 v97, v58, v96, v92\n"
  "v_fma_f32 v98, v59, v96, v92\n"
  "v_cvt_pk_bf16_f32 v79, v97, v98\n"
  "ds_write_b128 v251, v[76:79] offset:12288\n"
  "v_fma_f32 v93, v60, v96, v92\n"
  "v_fma_f32 v94, v61, v96, v92\n"
  "v_cvt_pk_bf16_f32 v240, v93, v94\n"
  "v_fma_f32 v97, v62, v96, v92\n"
  "v_fma_f32 v98, v63, v96, v92\n"
  "v_cvt_pk_bf16_f32 v241, v97, v98\n"
  "ds_write_b64 v247, v[240:241] offset:12288\n"
  "v_add_f32 v102, v102, v103\n"
  "v_add_f32 v100, v100, v102\n"
  "s_waitcnt lgkmcnt(15)\n"
  "v_add_f32 v104, v104, v105\n"
  "v_add_f32 v106, v106, v107\n"
  "v_add_f32 v104, v104, v106\n"
  "v_mul_f32 v100, 0xbbaaaaab, v100\n"
  "v_mul_f32 v104, 0x3baaaaab, v104\n"
  "v_fma_f32 v104, -v100, v100, v104\n"
  "v_add_f32_e64 v104, abs(v104), s43\n"
  "v_rsq_f32 v104, v104\n"
  "s_nop 0\n"
  "v_mul_f32 v100, v100, v104\n"
  "v_fma_f32 v101, v64, v104, v100\n"
  "v_fma_f32 v102, v65, v104, v100\n"
  "v_cvt_pk_bf16_f32 v4, v101, v102\n"
  "v_fma_f32 v105, v66, v104, v100\n"
  "v_fma_f32 v106, v67, v104, v100\n"
  "v_cvt_pk_bf16_f32 v5, v105, v106\n"
  "v_fma_f32 v101, v68, v104, v100\n"
  "v_fma_f32 v102, v69, v104, v100\n"
  "v_cvt_pk_bf16_f32 v6, v101, v102\n"
  "v_fma_f32 v105, v70, v104, v100\n"
  "v_fma_f32 v106, v71, v104, v100\n"
  "v_cvt_pk_bf16_f32 v7, v105, v106\n"
  "ds_write_b128 v251, v[4:7] offset:18432\n"
  "v_fma_f32 v101, v72, v104, v100\n"
  "v_fma_f32 v102, v73, v104, v100\n"
  "v_cvt_pk_bf16_f32 v240, v101, v102\n"
  "v_fma_f32 v105, v74, v104, v100\n"
  "v_fma_f32 v106, v75, v104, v100\n"
  "v_cvt_pk_bf16_f32 v241, v105, v106\n"
  "ds_write_b64 v247, v[240:241] offset:18432\n"
  "s_waitcnt vmcnt(6) lgkmcnt(0)\n"
  "s_barrier\n"
  "ds_read_b128 v[4:7], v252 offset:0\n"
  "ds_read_b128 v[76:79], v252 offset:1024\n"
  "ds_read_b128 v[80:83], v252 offset:2048\n"
  "s_add_u32 s86, s77, 147456\n"
  "s_add_u32 m0, s80, 49152\n"
  "s_add_u32 s87, s77, 184320\n"
  "ds_read_b64_tr_b16 a[0:1], v237 offset:0\n"
  "buffer_load_dwordx4 v199, s[28:31], s86 offen lds\n"
  "ds_read_b64_tr_b16 a[2:3], v237 offset:2048\n"
  "ds_read_b64_tr_b16 a[24:25], v198 offset:0\n"
  "s_add_u32 m0, s80, 53248\n"
  "ds_read_b64_tr_b16 a[26:27], v198 offset:2048\n"
  "s_add_u32 s86, s77, 147584\n"
  "ds_read_b64_tr_b16 a[48:49], v197 offset:0\n"
  "buffer_load_dwordx4 v199, s[28:31], s87 offen lds\n"
  "ds_read_b64_tr_b16 a[50:51], v197 offset:2048\n"
  "ds_read_b64_tr_b16 a[4:5], v237 offset:4096\n"
  "s_add_u32 m0, s80, 57344\n"
  "s_waitcnt lgkmcnt(9)\n"
  "v_mfma_f32_16x16x32_bf16 v[84:87], a[72:75], v[4:7], 0\n"
  "s_add_u32 s87, s77, 184448\n"
  "ds_read_b64_tr_b16 a[6:7], v237 offset:6144\n"
  "ds_read_b64_tr_b16 a[28:29], v198 offset:4096\n"
  "v_mfma_f32_16x16x32_bf16 v[88:91], a[96:99], v[4:7], 0\n"
  "s_waitcnt lgkmcnt(10)\n"
  "ds_read_b64_tr_b16 a[30:31], v198 offset:6144\n"
  "ds_read_b64_tr_b16 a[52:53], v197 offset:4096\n"
  "v_mfma_f32_16x16x32_bf16 v[92:95], a[120:123], v[4:7], 0\n"
  "s_waitcnt lgkmcnt(10)\n"
  "ds_read_b128 v[96:99], v252 offset:3072\n"
  "ds_read_b64_tr_b16 a[54:55], v197 offset:6144\n"
  "v_mfma_f32_16x16x32_bf16 v[84:87], a[76:79], v[76:79], v[84:87]\n"
  "s_waitcnt lgkmcnt(10)\n"
  "buffer_load_dwordx4 v199, s[28:31], s86 offen lds\n"
  "v_mfma_f32_16x16x32_bf16 v[88:91], a[100:103], v[76:79], v[88:91]\n"
  "s_add_u32 m0, s80, 61440\n"
  "s_add_u32 s86, s77, 147712\n"
  "v_mfma_f32_16x16x32_bf16 v[92:95], a[124:127], v[76:79], v[92:95]\n"
  "s_waitcnt lgkmcnt(6)\n"
  "ds_read_b128 v[4:7], v252 offset:4096\n"
  "v_mfma_f32_16x16x32_bf16 v[84:87], a[80:83], v[80:83], v[84:87]\n"
  "s_waitcnt lgkmcnt(5)\n"
  "buffer_load_dwordx4 v199, s[28:31], s87 offen lds\n"
  "v_mfma_f32_16x16x32_bf16 v[88:91], a[104:107], v[80:83], v[88:91]\n"
  "s_add_u32 m0, s80, 65536\n"
  "s_add_u32 s87, s77, 184576\n"
  "v_mfma_f32_16x16x32_bf16 v[92:95], a[128:131], v[80:83], v[92:95]\n"
  "s_waitcnt lgkmcnt(2)\n"
  "ds_read_b128 v[76:79], v252 offset:5120\n"
  "s_waitcnt vmcnt(4) lgkmcnt(2)\n"
  "s_barrier\n"
  "v_mfma_f32_16x16x32_bf16 v[84:87], a[84:87], v[96:99], v[84:87]\n"
  "s_waitcnt lgkmcnt(1)\n"
  "buffer_load_dwordx4 v199, s[28:31], s86 offen lds\n"
  "v_mfma_f32_16x16x32_bf16 v[88:91], a[108:111], v[96:99], v[88:91]\n"
  "s_add_u32 m0, s80, 69632\n"
  "ds_read_b64_tr_b16 a[8:9], v237 offset:24576\n"
  "ds_read_b64_tr_b16 a[10:11], v237 offset:26624\n"
  "v_mfma_f32_16x16x32_bf16 v[92:95], a[132:135], v[96:99], v[92:95]\n"
  "s_add_u32 s86, s76, 768\n"
  "ds_read_b128 v[80:83], v252 offset:6144\n"
  "ds_read_b64_tr_b16 a[32:33], v198 offset:24576\n"
  "v_mfma_f32_16x16x32_bf16 v[84:87], a[88:91], v[4:7], v[84:87]\n"
  "s_waitcnt lgkmcnt(4)\n"
  "ds_read_b64_tr_b16 a[34:35], v198 offset:26624\n"
  "ds_read_b64_tr_b16 a[56:57], v197 offset:24576\n"
  "v_mfma_f32_16x16x32_bf16 v[88:91], a[112:115], v[4:7], v[88:91]\n"
  "buffer_load_dwordx4 v199, s[28:31], s87 offen lds\n"
  "v_mfma_f32_16x16x32_bf16 v[92:95], a[136:139], v[4:7], v[92:95]\n"
  "s_add_u32 m0, s80, 0\n"
  "ds_read_b128 v[96:99], v252 offset:7168\n"
  "ds_read_b64_tr_b16 a[58:59], v197 offset:26624\n"
  "v_mfma_f32_16x16x32_bf16 v[84:87], a[92:95], v[76:79], v[84:87]\n"
  "s_add_u32 s87, s76, 19200\n"
  "ds_read_b64_tr_b16 a[12:13], v237 offset:28672\n"
  "ds_read_b64_tr_b16 a[14:15], v237 offset:30720\n"
  "v_mfma_f32_16x16x32_bf16 v[88:91], a[116:119], v[76:79], v[88:91]\n"
  "s_waitcnt lgkmcnt(7)\n"
  "ds_read_b64_tr_b16 a[36:37], v198 offset:28672\n"
  "ds_read_b64_tr_b16 a[38:39], v198 offset:30720\n"
  "v_mfma_f32_16x16x32_bf16 v[92:95], a[140:143], v[76:79], v[92:95]\n"
  "s_waitcnt lgkmcnt(7)\n"
  "ds_read_b128 v[4:7], v252 offset:8192\n"
  "ds_read_b64_tr_b16 a[60:61], v197 offset:28672\n"
  "v_mfma_f32_16x16x32_bf16 v[76:79], a[72:75], v[80:83], 0\n"
  "s_waitcnt lgkmcnt(8)\n"
  "ds_read_b64_tr_b16 a[62:63], v197 offset:30720\n"
  "buffer_load_dwordx4 v199, s[28:31], s86 offen lds\n"
  "v_mfma_f32_16x16x32_bf16 v[100:103], a[96:99], v[80:83], 0\n"
  "s_add_u32 m0, s80, 4096\n"
  "v_cvt_pk_bf16_f32 v84, v84, v85\n"
  "v_cvt_pk_bf16_f32 v85, v86, v87\n"
  "v_mfma_f32_16x16x32_bf16 v[104:107], a[120:123], v[80:83], 0\n"
  "s_add_u32 s86, s76, 896\n"
  "ds_read_b128 v[108:111], v252 offset:9216\n"
  "v_cvt_pk_bf16_f32 v86, v88, v89\n"
  "s_waitcnt lgkmcnt(9)\n"
  "v_mfma_f32_16x16x32_bf16 v[76:79], a[76:79], v[96:99], v[76:79]\n"
  "s_waitcnt lgkmcnt(4)\n"
  "buffer_load_dwordx4 v199, s[28:31], s87 offen lds\n"
  "v_mfma_f32_16x16x32_bf16 v[100:103], a[100:103], v[96:99], v[100:103]\n"
  "s_add_u32 m0, s80, 8192\n"
  "v_cvt_pk_bf16_f32 v87, v90, v91\n"
  "v_cvt_pk_bf16_f32 v232, v92, v93\n"
  "v_mfma_f32_16x16x32_bf16 v[104:107], a[124:127], v[96:99], v[104:107]\n"
  "s_add_u32 s87, s76, 19328\n"
  "ds_read_b128 v[80:83], v252 offset:10240\n"
  "v_cvt_pk_bf16_f32 v233, v94, v95\n"
  "s_waitcnt lgkmcnt(4)\n"
  "v_mfma_f32_16x16x32_bf16 v[76:79], a[80:83], v[4:7], v[76:79]\n"
  "s_waitcnt lgkmcnt(2)\n"
  "buffer_load_dwordx4 v199, s[28:31], s86 offen lds\n"
  "v_mfma_f32_16x16x32_bf16 v[100:103], a[104:107], v[4:7], v[100:103]\n"
  "s_add_u32 m0, s80, 12288\n"
  "s_add_u32 s86, s76, 1024\n"
  "v_mfma_f32_16x16x32_bf16 v[104:107], a[128:131], v[4:7], v[104:107]\n"
  "s_waitcnt lgkmcnt(1)\n"
  "ds_read_b128 v[88:91], v252 offset:11264\n"
  "v_mfma_f32_16x16x32_bf16 v[76:79], a[84:87], v[108:111], v[76:79]\n"
  "v_mfma_f32_16x16x32_bf16 v[100:103], a[108:111], v[108:111], v[100:103]\n"
  "s_waitcnt lgkmcnt(1)\n"
  "buffer_load_dwordx4 v199, s[28:31], s87 offen lds\n"
  "v_mfma_f32_16x16x32_bf16 v[104:107], a[132:135], v[108:111], v[104:107]\n"
  "s_add_u32 m0, s80, 16384\n"
  "ds_read_b128 v[4:7], v252 offset:12288\n"
  "s_add_u32 s87, s76, 19456\n"
  "v_mfma_f32_16x16x32_bf16 v[76:79], a[88:91], v[80:83], v[76:79]\n"
  "v_mfma_f32_16x16x32_bf16 v[100:103], a[112:115], v[80:83], v[100:103]\n"
  "s_waitcnt lgkmcnt(1)\n"
  "v_mfma_f32_16x16x32_bf16 v[104:107], a[136:139], v[80:83], v[104:107]\n"
  "ds_read_b128 v[92:95], v252 offset:13312\n"
  "buffer_load_dwordx4 v199, s[28:31], s86 offen lds\n"
  "v_mfma_f32_16x16x32_bf16 v[76:79], a[92:95], v[88:91], v[76:79]\n"
  "s_add_u32 m0, s80, 20480\n"
  "s_nop 0\n"
  "s_nop 0\n"
  "v_mfma_f32_16x16x32_bf16 v[100:103], a[116:119], v[88:91], v[100:103]\n"
  "s_waitcnt lgkmcnt(1)\n"
  "s_nop 0\n"
  "s_nop 0\n"
  "v_mfma_f32_16x16x32_bf16 v[104:107], a[140:143], v[88:91], v[104:107]\n"
  "ds_read_b128 v[80:83], v252 offset:14336\n"
  "v_cvt_pk_bf16_f32 v76, v76, v77\n"
  "v_mfma_f32_16x16x32_bf16 v[88:91], a[72:75], v[4:7], 0\n"
  "buffer_load_dwordx4 v199, s[28:31], s87 offen lds\n"
  "v_mfma_f32_16x16x32_bf16 v[96:99], a[96:99], v[4:7], 0\n"
  "s_waitcnt lgkmcnt(1)\n"
  "v_cvt_pk_bf16_f32 v77, v78, v79\n"
  "v_cvt_pk_bf16_f32 v78, v100, v101\n"
  "v_mfma_f32_16x16x32_bf16 v[108:111], a[120:123], v[4:7], 0\n"
  "ds_read_b128 v[112:115], v252 offset:15360\n"
  "v_cvt_pk_bf16_f32 v79, v102, v103\n"
  "v_mfma_f32_16x16x32_bf16 v[88:91], a[76:79], v[92:95], v[88:91]\n"
  "v_cvt_pk_bf16_f32 v194, v104, v105\n"
  "v_cvt_pk_bf16_f32 v195, v106, v107\n"
  "v_mfma_f32_16x16x32_bf16 v[96:99], a[100:103], v[92:95], v[96:99]\n"
  "s_waitcnt lgkmcnt(1)\n"
  "v_mfma_f32_16x16x32_bf16 v[108:111], a[124:127], v[92:95], v[108:111]\n"
  "ds_read_b128 v[4:7], v252 offset:16384\n"
  "v_mfma_f32_16x16x32_bf16 v[88:91], a[80:83], v[80:83], v[88:91]\n"
  "v_mfma_f32_16x16x32_bf16 v[96:99], a[104:107], v[80:83], v[96:99]\n"
  "s_waitcnt lgkmcnt(1)\n"
  "s_waitcnt vmcnt(6)\n"
  "s_barrier\n"
  "v_mfma_f32_16x16x32_bf16 v[108:111], a[128:131], v[80:83], v[108:111]\n"
  "s_add_u32 s86, s76, 37632\n"
  "ds_read_b128 v[92:95], v252 offset:17408\n"
  "ds_read_b64_tr_b16 a[16:17], v237 offset:49152\n"
  "v_mfma_f32_16x16x32_bf16 v[88:91], a[84:87], v[112:115], v[88:91]\n"
  "s_add_u32 m0, s80, 24576\n"
  "ds_read_b64_tr_b16 a[18:19], v237 offset:51200\n"
  "ds_read_b64_tr_b16 a[40:41], v198 offset:49152\n"
  "v_mfma_f32_16x16x32_bf16 v[96:99], a[108:111], v[112:115], v[96:99]\n"
  "s_add_u32 s87, s76, 56064\n"
  "ds_read_b64_tr_b16 a[42:43], v198 offset:51200\n"
  "ds_read_b64_tr_b16 a[64:65], v197 offset:49152\n"
  "v_mfma_f32_16x16x32_bf16 v[108:111], a[132:135], v[112:115], v[108:111]\n"
  "s_waitcnt lgkmcnt(6)\n"
  "ds_read_b64_tr_b16 a[66:67], v197 offset:51200\n"
  "ds_read_b64_tr_b16 a[20:21], v237 offset:53248\n"
  "v_mfma_f32_16x16x32_bf16 v[88:91], a[88:91], v[4:7], v[88:91]\n"
  "ds_read_b128 v[80:83], v252 offset:18432\n"
  "ds_read_b64_tr_b16 a[22:23], v237 offset:55296\n"
  "v_mfma_f32_16x16x32_bf16 v[96:99], a[112:115], v[4:7], v[96:99]\n"
  "s_waitcnt lgkmcnt(9)\n"
  "ds_read_b64_tr_b16 a[44:45], v198 offset:53248\n"
  "ds_read_b64_tr_b16 a[46:47], v198 offset:55296\n"
  "v_mfma_f32_16x16x32_bf16 v[108:111], a[136:139], v[4:7], v[108:111]\n"
  "s_waitcnt lgkmcnt(9)\n"
  "ds_read_b64_tr_b16 a[68:69], v197 offset:53248\n"
  "ds_read_b64_tr_b16 a[70:71], v197 offset:55296\n"
  "v_mfma_f32_16x16x32_bf16 v[88:91], a[92:95], v[92:95], v[88:91]\n"
  "s_waitcnt lgkmcnt(9)\n"
  "ds_read_b128 v[4:7], v252 offset:19456\n"
  "buffer_load_dwordx4 v199, s[28:31], s86 offen lds\n"
  "v_mfma_f32_16x16x32_bf16 v[96:99], a[116:119], v[92:95], v[96:99]\n"
  "s_add_u32 m0, s80, 28672\n"
  "s_add_u32 s86, s76, 37760\n"
  "s_nop 0\n"
  "v_mfma_f32_16x16x32_bf16 v[108:111], a[140:143], v[92:95], v[108:111]\n"
  "s_waitcnt lgkmcnt(7)\n"
  "ds_read_b128 v[100:103], v252 offset:20480\n"
  "buffer_load_dwordx4 v199, s[28:31], s87 offen lds\n"
  "s_waitcnt lgkmcnt(7)\n"
  "v_mfma_f32_16x16x32_bf16 v[228:231], a[72:75], v[80:83], 0\n"
  "s_add_u32 m0, s80, 32768\n"
  "v_cvt_pk_bf16_f32 v88, v88, v89\n"
  "v_cvt_pk_bf16_f32 v89, v90, v91\n"
  "v_mfma_f32_16x16x32_bf16 v[92:95], a[96:99], v[80:83], 0\n"
  "s_add_u32 s87, s76, 56192\n"
  "v_cvt_pk_bf16_f32 v90, v96, v97\n"
  "v_cvt_pk_bf16_f32 v91, v98, v99\n"
  "v_mfma_f32_16x16x32_bf16 v[104:107], a[120:123], v[80:83], 0\n"
  "s_waitcnt lgkmcnt(2)\n"
  "ds_read_b128 v[96:99], v252 offset:21504\n"
  "v_cvt_pk_bf16_f32 v240, v108, v109\n"
  "s_waitcnt lgkmcnt(2)\n"
  "v_mfma_f32_16x16x32_bf16 v[228:231], a[76:79], v[4:7], v[228:231]\n"
  "buffer_load_dwordx4 v199, s[28:31], s86 offen lds\n"
  "v_mfma_f32_16x16x32_bf16 v[92:95], a[100:103], v[4:7], v[92:95]\n"
  "s_add_u32 m0, s80, 36864\n"
  "v_cvt_pk_bf16_f32 v241, v110, v111\n"
  "s_add_u32 s86, s76, 37888\n"
  "v_mfma_f32_16x16x32_bf16 v[104:107], a[124:127], v[4:7], v[104:107]\n"
  "s_waitcnt lgkmcnt(1)\n"
  "ds_read_b128 v[80:83], v252 offset:22528\n"
  "v_mfma_f32_16x16x32_bf16 v[228:231], a[80:83], v[100:103], v[228:231]\n"
  "buffer_load_dwordx4 v199, s[28:31], s87 offen lds\n"
  "v_mfma_f32_16x16x32_bf16 v[92:95], a[104:107], v[100:103], v[92:95]\n"
  "s_add_u32 m0, s80, 40960\n"
  "s_add_u32 s87, s76, 56320\n"
  "v_mfma_f32_16x16x32_bf16 v[104:107], a[128:131], v[100:103], v[104:107]\n"
  "s_waitcnt lgkmcnt(1)\n"
  "ds_read_b128 v[4:7], v252 offset:23552\n"
  "v_mfma_f32_16x16x32_bf16 v[228:231], a[84:87], v[96:99], v[228:231]\n"
  "v_mfma_f32_16x16x32_bf16 v[92:95], a[108:111], v[96:99], v[92:95]\n"
  "s_waitcnt lgkmcnt(1)\n"
  "buffer_load_dwordx4 v199, s[28:31], s86 offen lds\n"
  "v_mfma_f32_16x16x32_bf16 v[104:107], a[132:135], v[96:99], v[104:107]\n"
  "s_add_u32 m0, s80, 45056\n"
  "s_nop 0\n"
  "s_waitcnt vmcnt(5)\n"
  "s_barrier\n"
  "v_mfma_f32_16x16x32_bf16 v[228:231], a[88:91], v[80:83], v[228:231]\n"
  "s_add_u32 s86, s77, 148224\n"
  "ds_read_b128 v[96:99], v252 offset:0\n"
  "ds_read_b64_tr_b16 a[144:145], v237 offset:0\n"
  "v_mfma_f32_16x16x32_bf16 v[92:95], a[112:115], v[80:83], v[92:95]\n"
  "s_waitcnt lgkmcnt(2)\n"
  "buffer_load_dwordx4 v199, s[28:31], s87 offen lds\n"
  "v_mfma_f32_16x16x32_bf16 v[104:107], a[136:139], v[80:83], v[104:107]\n"
  "s_add_u32 m0, s80, 49152\n"
  "ds_read_b64_tr_b16 a[146:147], v237 offset:2048\n"
  "ds_read_b64_tr_b16 a[168:169], v198 offset:0\n"
  "v_mfma_f32_16x16x32_bf16 v[228:231], a[92:95], v[4:7], v[228:231]\n"
  "s_add_u32 s87, s77, 185088\n"
  "ds_read_b128 v[80:83], v252 offset:1024\n"
  "ds_read_b64_tr_b16 a[170:171], v198 offset:2048\n"
  "v_mfma_f32_16x16x32_bf16 v[92:95], a[116:119], v[4:7], v[92:95]\n"
  "ds_read_b64_tr_b16 a[192:193], v197 offset:0\n"
  "ds_read_b64_tr_b16 a[194:195], v197 offset:2048\n"
  "v_mfma_f32_16x16x32_bf16 v[104:107], a[140:143], v[4:7], v[104:107]\n"
  "s_waitcnt lgkmcnt(7)\n"
  "ds_read_b128 v[100:103], v252 offset:2048\n"
  "ds_read_b64_tr_b16 a[148:149], v237 offset:4096\n"
  "v_mfma_f32_16x16x32_bf16 v[4:7], a[0:3], v[96:99], 0\n"
  "s_waitcnt lgkmcnt(8)\n"
  "ds_read_b64_tr_b16 a[150:151], v237 offset:6144\n"
  "ds_read_b64_tr_b16 a[172:173], v198 offset:4096\n"
  "v_mfma_f32_16x16x32_bf16 v[108:111], a[24:27], v[96:99], 0\n"
  "s_waitcnt lgkmcnt(8)\n"
  "ds_read_b64_tr_b16 a[174:175], v198 offset:6144\n"
  "ds_read_b64_tr_b16 a[196:197], v197 offset:4096\n"
  "v_mfma_f32_16x16x32_bf16 v[112:115], a[48:51], v[96:99], 0\n"
  "s_waitcnt lgkmcnt(9)\n"
  "ds_read_b128 v[116:119], v252 offset:3072\n"
  "buffer_load_dwordx4 v199, s[28:31], s86 offen lds\n"
  "v_mfma_f32_16x16x32_bf16 v[4:7], a[4:7], v[80:83], v[4:7]\n"
  "s_add_u32 m0, s80, 53248\n"
  "ds_read_b64_tr_b16 a[198:199], v197 offset:6144\n"
  "v_cvt_pk_bf16_f32 v228, v228, v229\n"
  "v_mfma_f32_16x16x32_bf16 v[108:111], a[28:31], v[80:83], v[108:111]\n"
  "s_add_u32 s86, s77, 148352\n"
  "buffer_load_dwordx4 v199, s[28:31], s87 offen lds\n"
  "v_mfma_f32_16x16x32_bf16 v[112:115], a[52:55], v[80:83], v[112:115]\n"
  "s_add_u32 m0, s80, 57344\n"
  "ds_read_b128 v[96:99], v252 offset:4096\n"
  "v_cvt_pk_bf16_f32 v229, v230, v231\n"
  "s_waitcnt lgkmcnt(8)\n"
  "v_mfma_f32_16x16x32_bf16 v[4:7], a[8:11], v[100:103], v[4:7]\n"
  "s_add_u32 s87, s77, 185216\n"
  "v_cvt_pk_bf16_f32 v230, v92, v93\n"
  "v_cvt_pk_bf16_f32 v231, v94, v95\n"
  "v_mfma_f32_16x16x32_bf16 v[108:111], a[32:35], v[100:103], v[108:111]\n"
  "s_waitcnt lgkmcnt(3)\n"
  "v_cvt_pk_bf16_f32 v238, v104, v105\n"
  "v_cvt_pk_bf16_f32 v239, v106, v107\n"
  "v_mfma_f32_16x16x32_bf16 v[112:115], a[56:59], v[100:103], v[112:115]\n"
  "s_waitcnt lgkmcnt(1)\n"
  "ds_read_b128 v[80:83], v252 offset:5120\n"
  "buffer_load_dwordx4 v199, s[28:31], s86 offen lds\n"
  "v_mfma_f32_16x16x32_bf16 v[4:7], a[12:15], v[116:119], v[4:7]\n"
  "s_add_u32 m0, s80, 61440\n"
  "s_add_u32 s86, s77, 148480\n"
  "v_mfma_f32_16x16x32_bf16 v[108:111], a[36:39], v[116:119], v[108:111]\n"
  "s_waitcnt lgkmcnt(1)\n"
  "v_mfma_f32_16x16x32_bf16 v[112:115], a[60:63], v[116:119], v[112:115]\n"
  "ds_read_b128 v[92:95], v252 offset:6144\n"
  "v_mfma_f32_16x16x32_bf16 v[4:7], a[16:19], v[96:99], v[4:7]\n"
  "buffer_load_dwordx4 v199, s[28:31], s87 offen lds\n"
  "v_mfma_f32_16x16x32_bf16 v[108:111], a[40:43], v[96:99], v[108:111]\n"
  "s_add_u32 m0, s80, 65536\n"
  "s_add_u32 s87, s77, 185344\n"
  "v_mfma_f32_16x16x32_bf16 v[112:115], a[64:67], v[96:99], v[112:115]\n"
  "s_waitcnt lgkmcnt(1)\n"
  "ds_read_b128 v[100:103], v252 offset:7168\n"
  "v_mfma_f32_16x16x32_bf16 v[4:7], a[20:23], v[80:83], v[4:7]\n"
  "s_nop 0\n"
  "s_nop 0\n"
  "v_mfma_f32_16x16x32_bf16 v[108:111], a[44:47], v[80:83], v[108:111]\n"
  "s_waitcnt lgkmcnt(1)\n"
  "buffer_load_dwordx4 v199, s[28:31], s86 offen lds\n"
  "v_mfma_f32_16x16x32_bf16 v[112:115], a[68:71], v[80:83], v[112:115]\n"
  "s_add_u32 m0, s80, 69632\n"
  "ds_read_b128 v[96:99], v252 offset:8192\n"
  "v_cvt_pk_bf16_f32 v4, v4, v5\n"
  "v_mfma_f32_16x16x32_bf16 v[116:119], a[0:3], v[92:95], 0\n"
  "v_cvt_pk_bf16_f32 v5, v6, v7\n"
  "v_cvt_pk_bf16_f32 v6, v108, v109\n"
  "v_mfma_f32_16x16x32_bf16 v[80:83], a[24:27], v[92:95], 0\n"
  "s_waitcnt lgkmcnt(1)\n"
  "v_cvt_pk_bf16_f32 v7, v110, v111\n"
  "v_cvt_pk_bf16_f32 v234, v112, v113\n"
  "v_mfma_f32_16x16x32_bf16 v[104:107], a[48:51], v[92:95], 0\n"
  "ds_read_b128 v[108:111], v252 offset:9216\n"
  "buffer_load_dwordx4 v199, s[28:31], s87 offen lds\n"
  "v_mfma_f32_16x16x32_bf16 v[116:119], a[4:7], v[100:103], v[116:119]\n"
  "v_cvt_pk_bf16_f32 v235, v114, v115\n"
  "v_mfma_f32_16x16x32_bf16 v[80:83], a[28:31], v[100:103], v[80:83]\n"
  "s_waitcnt lgkmcnt(1)\n"
  "v_mfma_f32_16x16x32_bf16 v[104:107], a[52:55], v[100:103], v[104:107]\n"
  "ds_read_b128 v[92:95], v252 offset:10240\n"
  "v_mfma_f32_16x16x32_bf16 v[116:119], a[8:11], v[96:99], v[116:119]\n"
  "v_mfma_f32_16x16x32_bf16 v[80:83], a[32:35], v[96:99], v[80:83]\n"
  "s_waitcnt lgkmcnt(1)\n"
  "s_waitcnt vmcnt(6)\n"
  "s_barrier\n"
  "v_mfma_f32_16x16x32_bf16 v[104:107], a[56:59], v[96:99], v[104:107]\n"
  "s_add_u32 s86, s78, 0\n"
  "ds_read_b128 v[100:103], v252 offset:11264\n"
  "ds_read_b64_tr_b16 a[152:153], v237 offset:24576\n"
  "v_mfma_f32_16x16x32_bf16 v[116:119], a[12:15], v[108:111], v[116:119]\n"
  "s_add_u32 m0, s80, 0\n"
  "ds_read_b64_tr_b16 a[154:155], v237 offset:26624\n"
  "ds_read_b64_tr_b16 a[176:177], v198 offset:24576\n"
  "v_mfma_f32_16x16x32_bf16 v[80:83], a[36:39], v[108:111], v[80:83]\n"
  "s_add_u32 s87, s78, 18432\n"
  "ds_read_b64_tr_b16 a[178:179], v198 offset:26624\n"
  "ds_read_b64_tr_b16 a[200:201], v197 offset:24576\n"
  "v_mfma_f32_16x16x32_bf16 v[104:107], a[60:63], v[108:111], v[104:107]\n"
  "s_waitcnt lgkmcnt(6)\n"
  "ds_read_b64_tr_b16 a[202:203], v197 offset:26624\n"
  "ds_read_b64_tr_b16 a[156:157], v237 offset:28672\n"
  "v_mfma_f32_16x16x32_bf16 v[116:119], a[16:19], v[92:95], v[116:119]\n"
  "ds_read_b64_tr_b16 a[158:159], v237 offset:30720\n"
  "ds_read_b64_tr_b16 a[180:181], v198 offset:28672\n"
  "v_mfma_f32_16x16x32_bf16 v[80:83], a[40:43], v[92:95], v[80:83]\n"
  "s_waitcnt lgkmcnt(9)\n"
  "ds_read_b64_tr_b16 a[182:183], v198 offset:30720\n"
  "ds_read_b64_tr_b16 a[204:205], v197 offset:28672\n"
  "v_mfma_f32_16x16x32_bf16 v[104:107], a[64:67], v[92:95], v[104:107]\n"
  "s_waitcnt lgkmcnt(9)\n"
  "ds_read_b64_tr_b16 a[206:207], v197 offset:30720\n"
  "buffer_load_dwordx4 v236, s[32:35], s86 offen lds\n"
  "v_mfma_f32_16x16x32_bf16 v[116:119], a[20:23], v[100:103], v[116:119]\n"
  "s_add_u32 m0, s80, 4096\n"
  "s_add_u32 s86, s78, 128\n"
  "s_nop 0\n"
  "v_mfma_f32_16x16x32_bf16 v[80:83], a[44:47], v[100:103], v[80:83]\n"
  "s_waitcnt lgkmcnt(6)\n"
  "buffer_load_dwordx4 v236, s[32:35], s87 offen lds\n"
  "v_mfma_f32_16x16x32_bf16 v[104:107], a[68:71], v[100:103], v[104:107]\n"
  "s_add_u32 m0, s80, 8192\n"
  "v_cvt_pk_bf16_f32 v116, v116, v117\n"
  "v_cvt_pk_bf16_f32 v117, v118, v119\n"
  "v_mfma_f32_16x16x32_bf16 v[92:95], v[84:87], v[4:7], 0\n"
  "s_add_u32 s87, s78, 18560\n"
  "v_cvt_pk_bf16_f32 v118, v80, v81\n"
  "v_cvt_pk_bf16_f32 v119, v82, v83\n"
  "v_mfma_f32_16x16x32_bf16 v[96:99], v[76:79], v[4:7], 0\n"
  "s_waitcnt lgkmcnt(0)\n"
  "v_cvt_pk_bf16_f32 v192, v104, v105\n"
  "v_cvt_pk_bf16_f32 v193, v106, v107\n"
  "v_mfma_f32_16x16x32_bf16 v[100:103], v[88:91], v[4:7], 0\n"
  "buffer_load_dwordx4 v236, s[32:35], s86 offen lds\n"
  "v_mfma_f32_16x16x32_bf16 v[156:159], v[228:231], v[4:7], v[8:11]\n"
  "s_add_u32 m0, s80, 12288\n"
  "s_add_u32 s86, s78, 256\n"
  "s_nop 0\n"
  "v_mfma_f32_16x16x16_bf16 v[92:95], v[232:233], v[234:235], v[92:95]\n"
  "s_nop 0\n"
  "s_nop 0\n"
  "v_mfma_f32_16x16x16_bf16 v[96:99], v[194:195], v[234:235], v[96:99]\n"
  "v_mfma_f32_16x16x16_bf16 v[100:103], v[240:241], v[234:235], v[100:103]\n"
  "buffer_load_dwordx4 v236, s[32:35], s87 offen lds\n"
  "v_mfma_f32_16x16x16_bf16 v[156:159], v[238:239], v[234:235], v[156:159]\n"
  "s_add_u32 m0, s80, 16384\n"
  "s_add_u32 s87, s78, 18688\n"
  "s_nop 0\n"
  "v_mfma_f32_16x16x32_bf16 v[184:187], v[84:87], v[116:119], 0\n"
  "s_nop 0\n"
  "s_nop 0\n"
  "v_mfma_f32_16x16x32_bf16 v[80:83], v[76:79], v[116:119], 0\n"
  "s_nop 0\n"
  "v_max3_f32 v199, v156, v92, v93\n"
  "v_mfma_f32_16x16x32_bf16 v[104:107], v[88:91], v[116:119], 0\n"
  "ds_read_b128 v[4:7], v252 offset:12288\n"
  "buffer_load_dwordx4 v236, s[32:35], s86 offen lds\n"
  "v_mfma_f32_16x16x32_bf16 v[188:191], v[228:231], v[116:119], v[8:11]\n"
  "s_add_u32 m0, s80, 20480\n"
  "v_max3_f32 v199, v199, v94, v95\n"
  "v_max3_f32 v199, v199, v96, v97\n"
  "v_mfma_f32_16x16x16_bf16 v[184:187], v[232:233], v[192:193], v[184:187]\n"
  "v_max3_f32 v199, v199, v98, v99\n"
  "v_max3_f32 v199, v199, v100, v101\n"
  "v_mfma_f32_16x16x16_bf16 v[80:83], v[194:195], v[192:193], v[80:83]\n"
  "ds_read_b128 v[108:111], v252 offset:13312\n"
  "v_max3_f32 v199, v199, v102, v103\n"
  "v_mfma_f32_16x16x16_bf16 v[104:107], v[240:241], v[192:193], v[104:107]\n"
  "buffer_load_dwordx4 v236, s[32:35], s87 offen lds\n"
  "v_mfma_f32_16x16x16_bf16 v[188:191], v[238:239], v[192:193], v[188:191]\n"
  "s_waitcnt lgkmcnt(1)\n"
  "ds_read_b128 v[112:115], v252 offset:14336\n"
  "s_waitcnt vmcnt(6)\n"
  "s_barrier\n"
  "v_mfma_f32_16x16x32_bf16 v[132:135], a[0:3], v[4:7], 0\n"
  "s_add_u32 s86, s78, 36864\n"
  "ds_read_b64_tr_b16 a[160:161], v237 offset:49152\n"
  "ds_read_b64_tr_b16 a[162:163], v237 offset:51200\n"
  "v_mfma_f32_16x16x32_bf16 v[116:119], a[24:27], v[4:7], 0\n"
  "s_add_u32 m0, s80, 24576\n"
  "ds_read_b64_tr_b16 a[184:185], v198 offset:49152\n"
  "buffer_load_dwordx4 v236, s[32:35], s86 offen lds\n"
  "v_mfma_f32_16x16x32_bf16 v[120:123], a[48:51], v[4:7], 0\n"
  "s_add_u32 s87, s78, 55296\n"
  "ds_read_b128 v[124:127], v252 offset:15360\n"
  "ds_read_b64_tr_b16 a[186:187], v198 offset:51200\n"
  "s_waitcnt lgkmcnt(6)\n"
  "v_mfma_f32_16x16x32_bf16 v[132:135], a[4:7], v[108:111], v[132:135]\n"
  "s_add_u32 m0, s80, 28672\n"
  "ds_read_b64_tr_b16 a[208:209], v197 offset:49152\n"
  "ds_read_b64_tr_b16 a[210:211], v197 offset:51200\n"
  "v_mfma_f32_16x16x32_bf16 v[116:119], a[28:31], v[108:111], v[116:119]\n"
  "s_add_u32 s86, s78, 36992\n"
  "ds_read_b64_tr_b16 a[164:165], v237 offset:53248\n"
  "buffer_load_dwordx4 v236, s[32:35], s87 offen lds\n"
  "v_mfma_f32_16x16x32_bf16 v[120:123], a[52:55], v[108:111], v[120:123]\n"
  "s_add_u32 m0, s80, 32768\n"
  "ds_read_b128 v[4:7], v252 offset:16384\n"
  "ds_read_b64_tr_b16 a[166:167], v237 offset:55296\n"
  "s_waitcnt lgkmcnt(10)\n"
  "v_mfma_f32_16x16x32_bf16 v[132:135], a[8:11], v[112:115], v[132:135]\n"
  "s_add_u32 s87, s78, 55424\n"
  "ds_read_b64_tr_b16 a[188:189], v198 offset:53248\n"
  "ds_read_b64_tr_b16 a[190:191], v198 offset:55296\n"
  "v_mfma_f32_16x16x32_bf16 v[116:119], a[32:35], v[112:115], v[116:119]\n"
  "s_waitcnt lgkmcnt(7)\n"
  "ds_read_b64_tr_b16 a[212:213], v197 offset:53248\n"
  "ds_read_b64_tr_b16 a[214:215], v197 offset:55296\n"
  "v_mfma_f32_16x16x32_bf16 v[120:123], a[56:59], v[112:115], v[120:123]\n"
  "s_waitcnt lgkmcnt(7)\n"
  "ds_read_b128 v[108:111], v252 offset:17408\n"
  "buffer_load_dwordx4 v236, s[32:35], s86 offen lds\n"
  "v_mfma_f32_16x16x32_bf16 v[132:135], a[12:15], v[124:127], v[132:135]\n"
  "s_add_u32 m0, s80, 36864\n"
  "s_add_u32 s86, s78, 37120\n"
  "v_max3_f32 v237, v188, v184, v185\n"
  "v_mfma_f32_16x16x32_bf16 v[116:119], a[36:39], v[124:127], v[116:119]\n"
  "s_waitcnt lgkmcnt(5)\n"
  "v_max3_f32 v237, v237, v186, v187\n"
  "v_max3_f32 v237, v237, v80, v81\n"
  "v_mfma_f32_16x16x32_bf16 v[120:123], a[60:63], v[124:127], v[120:123]\n"
  "s_waitcnt lgkmcnt(3)\n"
  "v_max3_f32 v237, v237, v82, v83\n"
  "v_max3_f32 v237, v237, v104, v105\n"
  "v_mfma_f32_16x16x32_bf16 v[132:135], a[16:19], v[4:7], v[132:135]\n"
  "s_waitcnt lgkmcnt(1)\n"
  "ds_read_b128 v[124:127], v252 offset:18432\n"
  "buffer_load_dwordx4 v236, s[32:35], s87 offen lds\n"
  "v_mfma_f32_16x16x32_bf16 v[116:119], a[40:43], v[4:7], v[116:119]\n"
  "s_add_u32 m0, s80, 40960\n"
  "s_add_u32 s87, s78, 55552\n"
  "v_max3_f32 v237, v237, v106, v107\n"
  "v_mfma_f32_16x16x32_bf16 v[120:123], a[64:67], v[4:7], v[120:123]\n"
  "s_waitcnt lgkmcnt(1)\n"
  "v_permlane16_swap_b32 v199, v237\n"
  "v_mfma_f32_16x16x32_bf16 v[132:135], a[20:23], v[108:111], v[132:135]\n"
  "ds_read_b128 v[4:7], v252 offset:19456\n"
  "buffer_load_dwordx4 v236, s[32:35], s86 offen lds\n"
  "v_mfma_f32_16x16x32_bf16 v[116:119], a[44:47], v[108:111], v[116:119]\n"
  "s_add_u32 m0, s80, 45056\n"
  "v_max_f32 v199, v199, v237\n"
  "v_mov_b32 v237, v199\n"
  "v_mfma_f32_16x16x32_bf16 v[120:123], a[68:71], v[108:111], v[120:123]\n"
  "s_waitcnt lgkmcnt(1)\n"
  "ds_read_b128 v[128:131], v252 offset:20480\n"
  "v_cvt_pk_bf16_f32 v132, v132, v133\n"
  "v_mfma_f32_16x16x32_bf16 v[112:115], a[0:3], v[124:127], 0\n"
  "v_cvt_pk_bf16_f32 v133, v134, v135\n"
  "v_cvt_pk_bf16_f32 v134, v116, v117\n"
  "v_mfma_f32_16x16x32_bf16 v[108:111], a[24:27], v[124:127], 0\n"
  "buffer_load_dwordx4 v236, s[32:35], s87 offen lds\n"
  "v_mfma_f32_16x16x32_bf16 v[136:139], a[48:51], v[124:127], 0\n"
  "s_add_u32 m0, s81, 51968\n"
  "ds_read_b128 v[140:143], v252 offset:21504\n"
  "v_add_u32 v235, 32, v254\n"
  "s_waitcnt lgkmcnt(2)\n"
  "v_mfma_f32_16x16x32_bf16 v[112:115], a[4:7], v[4:7], v[112:115]\n"
  "v_bitop3_b32 v234, v255, v235, s91 bitop3:0x78\n"
  "v_add_u32 v198, 48, v254\n"
  "v_mfma_f32_16x16x32_bf16 v[108:111], a[28:31], v[4:7], v[108:111]\n"
  "s_waitcnt lgkmcnt(1)\n"
  "v_bitop3_b32 v235, v255, v198, s91 bitop3:0x78\n"
  "v_add_u32 v197, 64, v254\n"
  "v_mfma_f32_16x16x32_bf16 v[136:139], a[52:55], v[4:7], v[136:139]\n"
  "ds_read_b128 v[124:127], v252 offset:22528\n"
  "s_mov_b64 exec, s[54:55]\n"
  "buffer_load_dwordx4 v234, s[20:23], s50 offen nt lds\n"
  "s_mov_b64 exec, -1\n"
  "v_mfma_f32_16x16x32_bf16 v[112:115], a[8:11], v[128:131], v[112:115]\n"
  "s_add_u32 m0, s81, 51968\n"
  "v_bitop3_b32 v234, v255, v197, s91 bitop3:0x78\n"
  "v_cvt_pk_bf16_f32 v135, v118, v119\n"
  "v_mfma_f32_16x16x32_bf16 v[108:111], a[32:35], v[128:131], v[108:111]\n"
  "s_waitcnt lgkmcnt(1)\n"
  "v_cvt_pk_bf16_f32 v196, v120, v121\n"
  "v_cvt_pk_bf16_f32 v197, v122, v123\n"
  "v_mfma_f32_16x16x32_bf16 v[136:139], a[56:59], v[128:131], v[136:139]\n"
  "ds_read_b128 v[4:7], v252 offset:23552\n"
  "s_mov_b64 exec, s[54:55]\n"
  "buffer_load_dwordx4 v235, s[20:23], s51 offen offset:3456 nt lds\n"
  "s_mov_b64 exec, -1\n"
  "v_mfma_f32_16x16x32_bf16 v[112:115], a[12:15], v[140:143], v[112:115]\n"
  "s_add_u32 m0, s81, 58880\n"
  "s_waitcnt vmcnt(8)\n"
  "s_barrier\n"
  "v_mfma_f32_16x16x32_bf16 v[108:111], a[36:39], v[140:143], v[108:111]\n"
  "s_waitcnt lgkmcnt(1)\n"
  "ds_read_b64_tr_b16 a[216:217], v15 offset:0\n"
  "ds_read_b64_tr_b16 a[218:219], v15 offset:2048\n"
  "v_mfma_f32_16x16x32_bf16 v[136:139], a[60:63], v[140:143], v[136:139]\n"
  "s_mov_b64 exec, s[54:55]\n"
  "buffer_load_dwordx4 v234, s[20:23], s52 offen nt lds\n"
  "s_mov_b64 exec, -1\n"
  "v_mfma_f32_16x16x32_bf16 v[112:115], a[16:19], v[124:127], v[112:115]\n"
  "s_waitcnt lgkmcnt(2)\n"
  "ds_read_b128 v[116:119], v252 offset:0\n"
  "ds_read_b64_tr_b16 a[232:233], v15 offset:8192\n"
  "v_mfma_f32_16x16x32_bf16 v[108:111], a[40:43], v[124:127], v[108:111]\n"
  "ds_read_b64_tr_b16 a[234:235], v15 offset:10240\n"
  "ds_read_b64_tr_b16 a[220:221], v15 offset:4096\n"
  "v_mfma_f32_16x16x32_bf16 v[136:139], a[64:67], v[124:127], v[136:139]\n"
  "s_waitcnt lgkmcnt(5)\n"
  "ds_read_b64_tr_b16 a[222:223], v15 offset:6144\n"
  "ds_read_b64_tr_b16 a[236:237], v15 offset:12288\n"
  "v_mfma_f32_16x16x32_bf16 v[112:115], a[20:23], v[4:7], v[112:115]\n"
  "s_waitcnt lgkmcnt(6)\n"
  "ds_read_b128 v[120:123], v252 offset:1024\n"
  "ds_read_b64_tr_b16 a[238:239], v15 offset:14336\n"
  "v_mfma_f32_16x16x32_bf16 v[108:111], a[44:47], v[4:7], v[108:111]\n"
  "v_add_u32 v255, 2688, v255\n"
  "v_add_u32 v235, 0, v254\n"
  "v_mfma_f32_16x16x32_bf16 v[136:139], a[68:71], v[4:7], v[136:139]\n"
  "s_add_u32 m0, s81, 0\n"
  "ds_read_b128 v[124:127], v252 offset:2048\n"
  "v_bitop3_b32 v234, v255, v235, s91 bitop3:0x78\n"
  "s_waitcnt lgkmcnt(8)\n"
  "v_mfma_f32_16x16x32_bf16 v[4:7], v[116:119], a[144:147], 0\n"
  "s_waitcnt lgkmcnt(6)\n"
  "s_mov_b64 exec, s[54:55]\n"
  "buffer_load_dwordx4 v234, s[20:23], s44 offen nt lds\n"
  "s_mov_b64 exec, -1\n"
  "v_mfma_f32_16x16x32_bf16 v[152:155], v[116:119], a[168:171], 0\n"
  "s_add_u32 m0, s81, 0\n"
  "v_add_u32 v235, 16, v254\n"
  "v_bitop3_b32 v234, v255, v235, s91 bitop3:0x78\n"
  "v_mfma_f32_16x16x32_bf16 v[160:163], v[116:119], a[192:195], 0\n"
  "s_waitcnt lgkmcnt(2)\n"
  "ds_read_b128 v[128:131], v252 offset:3072\n"
  "s_mov_b64 exec, s[54:55]\n"
  "buffer_load_dwordx4 v234, s[20:23], s45 offen offset:3456 nt lds\n"
  "s_mov_b64 exec, -1\n"
  "v_mfma_f32_16x16x32_bf16 v[4:7], v[120:123], a[148:151], v[4:7]\n"
  "s_add_u32 m0, s81, 6912\n"
  "v_add_u32 v235, 32, v254\n"
  "v_bitop3_b32 v234, v255, v235, s91 bitop3:0x78\n"
  "v_mfma_f32_16x16x32_bf16 v[152:155], v[120:123], a[172:175], v[152:155]\n"
  "s_waitcnt lgkmcnt(1)\n"
  "v_add_u32 v235, 48, v254\n"
  "v_bitop3_b32 v198, v255, v235, s91 bitop3:0x78\n"
  "v_mfma_f32_16x16x32_bf16 v[160:163], v[120:123], a[196:199], v[160:163]\n"
  "ds_read_b128 v[116:119], v252 offset:4096\n"
  "s_mov_b64 exec, s[54:55]\n"
  "buffer_load_dwordx4 v234, s[20:23], s46 offen nt lds\n"
  "s_mov_b64 exec, -1\n"
  "v_mfma_f32_16x16x32_bf16 v[4:7], v[124:127], a[152:155], v[4:7]\n"
  "s_add_u32 m0, s81, 6912\n"
  "v_add_u32 v235, 64, v254\n"
  "v_bitop3_b32 v234, v255, v235, s91 bitop3:0x78\n"
  "v_mfma_f32_16x16x32_bf16 v[152:155], v[124:127], a[176:179], v[152:155]\n"
  "s_waitcnt lgkmcnt(1)\n"
  "v_add_u32 v235, 80, v254\n"
  "v_bitop3_b32 v193, v255, v235, s91 bitop3:0x78\n"
  "v_mfma_f32_16x16x32_bf16 v[160:163], v[124:127], a[200:203], v[160:163]\n"
  "ds_read_b128 v[120:123], v252 offset:5120\n"
  "s_mov_b64 exec, s[54:55]\n"
  "buffer_load_dwordx4 v198, s[20:23], s47 offen offset:3456 nt lds\n"
  "s_mov_b64 exec, -1\n"
  "v_mfma_f32_16x16x32_bf16 v[4:7], v[128:131], a[156:159], v[4:7]\n"
  "s_add_u32 m0, s81, 13824\n"
  "v_add_u32 v235, 96, v254\n"
  "v_bitop3_b32 v198, v255, v235, s91 bitop3:0x78\n"
  "v_mfma_f32_16x16x32_bf16 v[152:155], v[128:131], a[180:183], v[152:155]\n"
  "s_waitcnt lgkmcnt(1)\n"
  "v_cvt_pk_bf16_f32 v112, v112, v113\n"
  "v_cvt_pk_bf16_f32 v113, v114, v115\n"
  "v_mfma_f32_16x16x32_bf16 v[160:163], v[128:131], a[204:207], v[160:163]\n"
  "v_cvt_pk_bf16_f32 v114, v108, v109\n"
  "v_cvt_pk_bf16_f32 v115, v110, v111\n"
  "v_mfma_f32_16x16x32_bf16 v[4:7], v[116:119], a[160:163], v[4:7]\n"
  "ds_read_b128 v[124:127], v252 offset:6144\n"
  "s_mov_b64 exec, s[54:55]\n"
  "buffer_load_dwordx4 v234, s[20:23], s48 offen nt lds\n"
  "s_mov_b64 exec, -1\n"
  "v_mfma_f32_16x16x32_bf16 v[152:155], v[116:119], a[184:187], v[152:155]\n"
  "s_add_u32 m0, s81, 13824\n"
  "v_add_u32 v235, 112, v254\n"
  "v_bitop3_b32 v192, v255, v235, s91 bitop3:0x78\n"
  "v_mfma_f32_16x16x32_bf16 v[160:163], v[116:119], a[208:211], v[160:163]\n"
  "s_waitcnt lgkmcnt(1)\n"
  "v_cvt_pk_bf16_f32 v234, v136, v137\n"
  "v_cvt_pk_bf16_f32 v235, v138, v139\n"
  "v_mfma_f32_16x16x32_bf16 v[4:7], v[120:123], a[164:167], v[4:7]\n"
  "ds_read_b128 v[116:119], v252 offset:7168\n"
  "s_mov_b64 exec, s[54:55]\n"
  "buffer_load_dwordx4 v193, s[20:23], s49 offen offset:3456 nt lds\n"
  "s_mov_b64 exec, -1\n"
  "v_mfma_f32_16x16x32_bf16 v[152:155], v[120:123], a[188:191], v[152:155]\n"
  "s_add_u32 m0, s81, 20736\n"
  "v_add_u32 v193, 0, v254\n"
  "v_bitop3_b32 v183, v255, v193, s91 bitop3:0x78\n"
  "v_mfma_f32_16x16x32_bf16 v[160:163], v[120:123], a[212:215], v[160:163]\n"
  "s_waitcnt lgkmcnt(1)\n"
  "ds_read_b128 v[128:131], v252 offset:8192\n"
  "v_cvt_pk_bf16_f32 v110, v4, v5\n"
  "v_mfma_f32_16x16x32_bf16 v[164:167], v[124:127], a[144:147], 0\n"
  "s_mov_b64 exec, s[54:55]\n"
  "buffer_load_dwordx4 v198, s[20:23], s50 offen nt lds\n"
  "s_mov_b64 exec, -1\n"
  "v_mfma_f32_16x16x32_bf16 v[168:171], v[124:127], a[168:171], 0\n"
  "s_add_u32 m0, s81, 20736\n"
  "v_permlane32_swap_b32 v199, v237\n"
  "v_mfma_f32_16x16x32_bf16 v[172:175], v[124:127], a[192:195], 0\n"
  "s_waitcnt lgkmcnt(1)\n"
  "ds_read_b128 v[120:123], v252 offset:9216\n"
  "v_max_f32 v199, v199, v237\n"
  "v_mfma_f32_16x16x32_bf16 v[164:167], v[116:119], a[148:151], v[164:167]\n"
  "v_mul_f32 v199, 0xbe553b95, v199\n"
  "v_mov_b32 v237, v199\n"
  "v_mfma_f32_16x16x32_bf16 v[168:171], v[116:119], a[172:175], v[168:171]\n"
  "s_waitcnt lgkmcnt(1)\n"
  "s_mov_b64 exec, s[54:55]\n"
  "buffer_load_dwordx4 v192, s[20:23], s51 offen offset:3456 nt lds\n"
  "s_mov_b64 exec, -1\n"
  "v_mfma_f32_16x16x32_bf16 v[172:175], v[116:119], a[196:199], v[172:175]\n"
  "s_add_u32 m0, s81, 27648\n"
  "ds_read_b128 v[124:127], v252 offset:10240\n"
  "v_cvt_pk_bf16_f32 v111, v6, v7\n"
  "v_mfma_f32_16x16x32_bf16 v[164:167], v[128:131], a[152:155], v[164:167]\n"
  "v_permlane16_swap_b32 v199, v237\n"
  "v_mfma_f32_16x16x32_bf16 v[168:171], v[128:131], a[176:179], v[168:171]\n"
  "s_waitcnt lgkmcnt(1)\n"
  "s_mov_b64 exec, s[54:55]\n"
  "buffer_load_dwordx4 v183, s[20:23], s52 offen nt lds\n"
  "s_mov_b64 exec, -1\n"
  "v_mfma_f32_16x16x32_bf16 v[172:175], v[128:131], a[200:203], v[172:175]\n"
  "s_add_u32 s86, s79, 12288\n"
  "ds_read_b128 v[4:7], v252 offset:11264\n"
  "v_fmamk_f32 v96, v96, 0x3e553b95, v199\n"
  "v_mfma_f32_16x16x32_bf16 v[164:167], v[120:123], a[156:159], v[164:167]\n"
  "s_add_u32 m0, s80, 49152\n"
  "v_exp_f32 v96, v96\n"
  "v_mfma_f32_16x16x32_bf16 v[168:171], v[120:123], a[180:183], v[168:171]\n"
  "s_add_u32 s87, s79, 49152\n"
  "v_fmamk_f32 v97, v97, 0x3e553b95, v199\n"
  "v_fmamk_f32 v98, v98, 0x3e553b95, v199\n"
  "v_mfma_f32_16x16x32_bf16 v[172:175], v[120:123], a[204:207], v[172:175]\n"
  "s_waitcnt lgkmcnt(1)\n"
  "buffer_load_dwordx4 v236, s[32:35], s86 offen lds\n"
  "v_mfma_f32_16x16x32_bf16 v[164:167], v[124:127], a[160:163], v[164:167]\n"
  "s_add_u32 m0, s80, 53248\n"
  "v_exp_f32 v97, v97\n"
  "v_mfma_f32_16x16x32_bf16 v[168:171], v[124:127], a[184:187], v[168:171]\n"
  "s_add_u32 s86, s79, 12416\n"
  "v_exp_f32 v98, v98\n"
  "v_mfma_f32_16x16x32_bf16 v[172:175], v[124:127], a[208:211], v[172:175]\n"
  "s_waitcnt lgkmcnt(0)\n"
  "v_cvt_pk_bf16_f32 v96, v96, v97\n"
  "v_fmamk_f32 v99, v99, 0x3e553b95, v199\n"
  "v_mfma_f32_16x16x32_bf16 v[164:167], v[4:7], a[164:167], v[164:167]\n"
  "buffer_load_dwordx4 v236, s[32:35], s87 offen lds\n"
  "v_mfma_f32_16x16x32_bf16 v[168:171], v[4:7], a[188:191], v[168:171]\n"
  "s_add_u32 m0, s80, 57344\n"
  "v_exp_f32 v99, v99\n"
  "v_mfma_f32_16x16x32_bf16 v[172:175], v[4:7], a[212:215], v[172:175]\n"
  "s_add_u32 s87, s79, 49280\n"
  "v_cvt_pk_bf16_f32 v97, v98, v99\n"
  "v_fmamk_f32 v92, v92, 0x3e553b95, v199\n"
  "v_mfma_f32_16x16x32_bf16 v[116:119], v[84:87], v[132:135], 0\n"
  "v_exp_f32 v92, v92\n"
  "v_mfma_f32_16x16x32_bf16 v[120:123], v[76:79], v[132:135], 0\n"
  "buffer_load_dwordx4 v236, s[32:35], s86 offen lds\n"
  "v_mfma_f32_16x16x32_bf16 v[124:127], v[88:91], v[132:135], 0\n"
  "s_add_u32 m0, s80, 61440\n"
  "v_fmamk_f32 v93, v93, 0x3e553b95, v199\n"
  "v_fmamk_f32 v94, v94, 0x3e553b95, v199\n"
  "v_mfma_f32_16x16x32_bf16 v[128:131], v[228:231], v[132:135], v[8:11]\n"
  "s_add_u32 s86, s79, 12544\n"
  "v_exp_f32 v93, v93\n"
  "v_mfma_f32_16x16x16_bf16 v[116:119], v[232:233], v[196:197], v[116:119]\n"
  "v_exp_f32 v94, v94\n"
  "v_mfma_f32_16x16x16_bf16 v[120:123], v[194:195], v[196:197], v[120:123]\n"
  "buffer_load_dwordx4 v236, s[32:35], s87 offen lds\n"
  "v_mfma_f32_16x16x16_bf16 v[124:127], v[240:241], v[196:197], v[124:127]\n"
  "s_add_u32 m0, s80, 65536\n"
  "v_cvt_pk_bf16_f32 v98, v92, v93\n"
  "v_fmamk_f32 v95, v95, 0x3e553b95, v199\n"
  "v_mfma_f32_16x16x16_bf16 v[128:131], v[238:239], v[196:197], v[128:131]\n"
  "s_add_u32 s87, s79, 49408\n"
  "v_exp_f32 v95, v95\n"
  "v_mfma_f32_16x16x32_bf16 v[132:135], v[84:87], v[112:115], 0\n"
  "v_cvt_pk_bf16_f32 v99, v94, v95\n"
  "v_cvt_pk_bf16_f32 v108, v164, v165\n"
  "v_mfma_f32_16x16x32_bf16 v[136:139], v[76:79], v[112:115], 0\n"
  "ds_read_b128 v[4:7], v252 offset:12288\n"
  "buffer_load_dwordx4 v236, s[32:35], s86 offen lds\n"
  "v_mfma_f32_16x16x32_bf16 v[140:143], v[88:91], v[112:115], 0\n"
  "s_add_u32 m0, s80, 69632\n"
  "v_cvt_pk_bf16_f32 v109, v166, v167\n"
  "v_cvt_pk_bf16_f32 v146, v152, v153\n"
  "v_mfma_f32_16x16x32_bf16 v[148:151], v[228:231], v[112:115], v[8:11]\n"
  "v_cvt_pk_bf16_f32 v147, v154, v155\n"
  "v_cvt_pk_bf16_f32 v144, v168, v169\n"
  "v_mfma_f32_16x16x16_bf16 v[132:135], v[232:233], v[234:235], v[132:135]\n"
  "ds_read_b128 v[76:79], v252 offset:13312\n"
  "buffer_load_dwordx4 v236, s[32:35], s87 offen lds\n"
  "v_mfma_f32_16x16x16_bf16 v[136:139], v[194:195], v[234:235], v[136:139]\n"
  "v_cvt_pk_bf16_f32 v145, v170, v171\n"
  "v_cvt_pk_bf16_f32 v154, v160, v161\n"
  "v_mfma_f32_16x16x16_bf16 v[140:143], v[240:241], v[234:235], v[140:143]\n"
  "s_waitcnt lgkmcnt(1)\n"
  "v_cvt_pk_bf16_f32 v155, v162, v163\n"
  "v_cvt_pk_bf16_f32 v152, v172, v173\n"
  "v_mfma_f32_16x16x16_bf16 v[148:151], v[238:239], v[234:235], v[148:151]\n"
  "ds_read_b128 v[84:87], v252 offset:14336\n"
  "v_cvt_pk_bf16_f32 v153, v174, v175\n"
  "v_mfma_f32_16x16x32_bf16 v[88:91], v[4:7], a[144:147], 0\n"
  "v_fmamk_f32 v241, v156, 0x3e553b95, v199\n"
  "v_fmamk_f32 v100, v100, 0x3e553b95, v199\n"
  "v_mfma_f32_16x16x32_bf16 v[92:95], v[4:7], a[168:171], 0\n"
  "s_waitcnt lgkmcnt(1)\n"
  "v_exp_f32 v241, v241\n"
  "v_mfma_f32_16x16x32_bf16 v[168:171], v[4:7], a[192:195], 0\n"
  "ds_read_b128 v[156:159], v252 offset:15360\n"
  "v_fmamk_f32 v101, v101, 0x3e553b95, v199\n"
  "v_mfma_f32_16x16x32_bf16 v[88:91], v[76:79], a[148:151], v[88:91]\n"
  "v_exp_f32 v100, v100\n"
  "v_mfma_f32_16x16x32_bf16 v[92:95], v[76:79], a[172:175], v[92:95]\n"
  "s_waitcnt lgkmcnt(1)\n"
  "v_exp_f32 v101, v101\n"
  "v_mfma_f32_16x16x32_bf16 v[168:171], v[76:79], a[196:199], v[168:171]\n"
  "ds_read_b128 v[4:7], v252 offset:16384\n"
  "v_cvt_pk_bf16_f32 v100, v100, v101\n"
  "v_mfma_f32_16x16x32_bf16 v[88:91], v[84:87], a[152:155], v[88:91]\n"
  "v_fmamk_f32 v102, v102, 0x3e553b95, v199\n"
  "v_fmamk_f32 v103, v103, 0x3e553b95, v199\n"
  "v_mfma_f32_16x16x32_bf16 v[92:95], v[84:87], a[176:179], v[92:95]\n"
  "s_waitcnt lgkmcnt(1)\n"
  "v_exp_f32 v102, v102\n"
  "v_mfma_f32_16x16x32_bf16 v[168:171], v[84:87], a[200:203], v[168:171]\n"
  "ds_read_b128 v[76:79], v252 offset:17408\n"
  "v_fmamk_f32 v80, v80, 0x3e553b95, v237\n"
  "v_mfma_f32_16x16x32_bf16 v[88:91], v[156:159], a[156:159], v[88:91]\n"
  "v_exp_f32 v103, v103\n"
  "v_mfma_f32_16x16x32_bf16 v[92:95], v[156:159], a[180:183], v[92:95]\n"
  "s_waitcnt lgkmcnt(1)\n"
  "v_exp_f32 v80, v80\n"
  "v_mfma_f32_16x16x32_bf16 v[168:171], v[156:159], a[204:207], v[168:171]\n"
  "ds_read_b128 v[164:167], v252 offset:18432\n"
  "v_cvt_pk_bf16_f32 v101, v102, v103\n"
  "v_mfma_f32_16x16x32_bf16 v[88:91], v[4:7], a[160:163], v[88:91]\n"
  "v_cvt_pk_bf16_f32 v102, v241, 0\n"
  "v_mov_b32 v103, 0\n"
  "v_mfma_f32_16x16x32_bf16 v[92:95], v[4:7], a[184:187], v[92:95]\n"
  "s_waitcnt lgkmcnt(1)\n"
  "v_fmamk_f32 v81, v81, 0x3e553b95, v237\n"
  "v_fmamk_f32 v82, v82, 0x3e553b95, v237\n"
  "v_mfma_f32_16x16x32_bf16 v[168:171], v[4:7], a[208:211], v[168:171]\n"
  "ds_read_b128 v[172:175], v252 offset:19456\n"
  "v_fmamk_f32 v83, v83, 0x3e553b95, v237\n"
  "v_mfma_f32_16x16x32_bf16 v[88:91], v[76:79], a[164:167], v[88:91]\n"
  "v_exp_f32 v81, v81\n"
  "v_mfma_f32_16x16x32_bf16 v[92:95], v[76:79], a[188:191], v[92:95]\n"
  "s_waitcnt lgkmcnt(1)\n"
  "v_exp_f32 v82, v82\n"
  "v_mfma_f32_16x16x32_bf16 v[168:171], v[76:79], a[212:215], v[168:171]\n"
  "ds_read_b128 v[4:7], v252 offset:20480\n"
  "v_cvt_pk_bf16_f32 v80, v80, v81\n"
  "v_mfma_f32_16x16x32_bf16 v[192:195], v[164:167], a[144:147], 0\n"
  "v_exp_f32 v83, v83\n"
  "v_mfma_f32_16x16x32_bf16 v[84:87], v[164:167], a[168:171], 0\n"
  "s_waitcnt lgkmcnt(1)\n"
  "v_cvt_pk_bf16_f32 v156, v88, v89\n"
  "v_cvt_pk_bf16_f32 v157, v90, v91\n"
  "v_mfma_f32_16x16x32_bf16 v[196:199], v[164:167], a[192:195], 0\n"
  "ds_read_b128 v[76:79], v252 offset:21504\n"
  "v_cvt_pk_bf16_f32 v160, v92, v93\n"
  "v_mfma_f32_16x16x32_bf16 v[192:195], v[172:175], a[148:151], v[192:195]\n"
  "v_cvt_pk_bf16_f32 v161, v94, v95\n"
  "v_cvt_pk_bf16_f32 v164, v168, v169\n"
  "v_mfma_f32_16x16x32_bf16 v[84:87], v[172:175], a[172:175], v[84:87]\n"
  "s_waitcnt lgkmcnt(1)\n"
  "v_cvt_pk_bf16_f32 v165, v170, v171\n"
  "v_cvt_pk_bf16_f32 v81, v82, v83\n"
  "v_mfma_f32_16x16x32_bf16 v[196:199], v[172:175], a[196:199], v[196:199]\n"
  "ds_read_b128 v[88:91], v252 offset:22528\n"
  "v_fmamk_f32 v184, v184, 0x3e553b95, v237\n"
  "v_mfma_f32_16x16x32_bf16 v[192:195], v[4:7], a[152:155], v[192:195]\n"
  "v_exp_f32 v184, v184\n"
  "v_mfma_f32_16x16x32_bf16 v[84:87], v[4:7], a[176:179], v[84:87]\n"
  "s_waitcnt lgkmcnt(1)\n"
  "v_add_u32 v241, 16384, v15\n"
  "v_fmamk_f32 v185, v185, 0x3e553b95, v237\n"
  "v_mfma_f32_16x16x32_bf16 v[196:199], v[4:7], a[200:203], v[196:199]\n"
  "ds_read_b128 v[92:95], v252 offset:23552\n"
  "ds_read_b64_tr_b16 a[244:245], v241 offset:0\n"
  "v_mfma_f32_16x16x32_bf16 v[192:195], v[76:79], a[156:159], v[192:195]\n"
  "ds_read_b64_tr_b16 a[246:247], v241 offset:2048\n"
  "ds_read_b64_tr_b16 a[248:249], v241 offset:4096\n"
  "v_mfma_f32_16x16x32_bf16 v[84:87], v[76:79], a[180:183], v[84:87]\n"
  "s_waitcnt lgkmcnt(4)\n"
  "ds_read_b64_tr_b16 a[250:251], v241 offset:6144\n"
  "v_fmamk_f32 v186, v186, 0x3e553b95, v237\n"
  "v_mfma_f32_16x16x32_bf16 v[196:199], v[76:79], a[204:207], v[196:199]\n"
  "v_exp_f32 v185, v185\n"
  "v_mfma_f32_16x16x32_bf16 v[192:195], v[88:91], a[160:163], v[192:195]\n"
  "v_exp_f32 v186, v186\n"
  "v_mfma_f32_16x16x32_bf16 v[84:87], v[88:91], a[184:187], v[84:87]\n"
  "s_waitcnt lgkmcnt(4)\n"
  "v_cvt_pk_bf16_f32 v82, v184, v185\n"
  "v_fmamk_f32 v187, v187, 0x3e553b95, v237\n"
  "v_mfma_f32_16x16x32_bf16 v[196:199], v[88:91], a[208:211], v[196:199]\n"
  "s_waitcnt lgkmcnt(2)\n"
  "v_exp_f32 v187, v187\n"
  "v_mfma_f32_16x16x32_bf16 v[192:195], v[92:95], a[164:167], v[192:195]\n"
  "s_waitcnt lgkmcnt(0)\n"
  "s_waitcnt vmcnt(18)\n"
  "s_barrier\n"
  "v_mfma_f32_16x16x32_bf16 v[84:87], v[92:95], a[188:191], v[84:87]\n"
  "ds_read_b64_tr_b16 a[224:225], v15 offset:24576\n"
  "ds_read_b64_tr_b16 a[226:227], v15 offset:26624\n"
  "v_mfma_f32_16x16x32_bf16 v[196:199], v[92:95], a[212:215], v[196:199]\n"
  "ds_read_b64_tr_b16 a[240:241], v15 offset:32768\n"
  "ds_read_b64_tr_b16 a[242:243], v15 offset:34816\n"
  "v_mfma_f32_16x16x32_bf16 v[168:171], v[16:19], v[96:99], 0\n"
  "ds_read_b64_tr_b16 a[252:253], v241 offset:24576\n"
  "ds_read_b64_tr_b16 a[254:255], v241 offset:26624\n"
  "v_mfma_f32_16x16x32_bf16 v[172:175], v[108:111], v[96:99], 0\n"
  "ds_read_b64_tr_b16 a[228:229], v15 offset:28672\n"
  "ds_read_b64_tr_b16 a[230:231], v15 offset:30720\n"
  "v_mfma_f32_16x16x32_bf16 v[176:179], v[144:147], v[96:99], 0\n"
  "ds_read_b64_tr_b16 v[4:5], v15 offset:36864\n"
  "ds_read_b64_tr_b16 v[6:7], v15 offset:38912\n"
  "v_mfma_f32_16x16x32_bf16 v[180:183], v[152:155], v[96:99], 0\n"
  "s_waitcnt lgkmcnt(8)\n"
  "ds_read_b64_tr_b16 v[76:77], v241 offset:28672\n"
  "ds_read_b64_tr_b16 v[78:79], v241 offset:30720\n"
  "v_mfma_f32_16x16x32_bf16 v[168:171], v[16:19], v[100:103], v[168:171]\n"
  "s_waitcnt lgkmcnt(8)\n"
  "v_cvt_pk_bf16_f32 v158, v192, v193\n"
  "v_cvt_pk_bf16_f32 v159, v194, v195\n"
  "v_cvt_pk_bf16_f32 v162, v84, v85\n"
  "v_cvt_pk_bf16_f32 v163, v86, v87\n"
  "v_mfma_f32_16x16x32_bf16 v[172:175], v[156:159], v[100:103], v[172:175]\n"
  "s_waitcnt lgkmcnt(5)\n"
  "v_cvt_pk_bf16_f32 v166, v196, v197\n"
  "v_cvt_pk_bf16_f32 v167, v198, v199\n"
  "v_mfma_f32_16x16x32_bf16 v[176:179], v[160:163], v[100:103], v[176:179]\n"
  "s_waitcnt lgkmcnt(3)\n"
  "v_cvt_pk_bf16_f32 v83, v186, v187\n"
  "v_fmamk_f32 v240, v188, 0x3e553b95, v237\n"
  "v_mfma_f32_16x16x32_bf16 v[180:183], v[164:167], v[100:103], v[180:183]\n"
  "s_waitcnt lgkmcnt(1)\n"
  "v_exp_f32 v240, v240\n"
  "v_mfma_f32_16x16x32_bf16 v[184:187], v[16:19], v[80:83], 0\n"
  "s_waitcnt lgkmcnt(0)\n"
  "v_fmamk_f32 v104, v104, 0x3e553b95, v237\n"
  "v_fmamk_f32 v105, v105, 0x3e553b95, v237\n"
  "v_mfma_f32_16x16x32_bf16 v[188:191], v[108:111], v[80:83], 0\n"
  "v_exp_f32 v104, v104\n"
  "v_mfma_f32_16x16x32_bf16 v[192:195], v[144:147], v[80:83], 0\n"
  "v_exp_f32 v105, v105\n"
  "v_mfma_f32_16x16x32_bf16 v[196:199], v[152:155], v[80:83], 0\n"
  "v_cvt_pk_bf16_f32 v104, v104, v105\n"
  "v_fmamk_f32 v106, v106, 0x3e553b95, v237\n"
  "v_exp_f32 v106, v106\n"
  "v_fmamk_f32 v107, v107, 0x3e553b95, v237\n"
  "v_exp_f32 v107, v107\n"
  "s_waitcnt vmcnt(0)\n"
  "s_barrier\n"
  "ds_read_b64_tr_b16 v[80:81], v15 offset:49152\n"
  "ds_read_b64_tr_b16 v[82:83], v15 offset:51200\n"
  "ds_read_b64_tr_b16 v[84:85], v15 offset:57344\n"
  "ds_read_b64_tr_b16 v[86:87], v15 offset:59392\n"
  "ds_read_b64_tr_b16 v[88:89], v241 offset:49152\n"
  "ds_read_b64_tr_b16 v[90:91], v241 offset:51200\n"
  "ds_read_b64_tr_b16 v[92:93], v15 offset:53248\n"
  "v_cvt_pk_bf16_f32 v105, v106, v107\n"
  "v_cvt_pk_bf16_f32 v106, v240, 0\n"
  "v_mov_b32 v107, 0\n"
  "ds_read_b64_tr_b16 v[94:95], v15 offset:55296\n"
  "ds_read_b64_tr_b16 v[96:97], v15 offset:61440\n"
  "v_mfma_f32_16x16x32_bf16 v[184:187], v[16:19], v[104:107], v[184:187]\n"
  "ds_read_b64_tr_b16 v[98:99], v15 offset:63488\n"
  "ds_read_b64_tr_b16 v[100:101], v241 offset:53248\n"
  "v_mfma_f32_16x16x32_bf16 v[188:191], v[156:159], v[104:107], v[188:191]\n"
  "s_waitcnt lgkmcnt(9)\n"
  "ds_read_b64_tr_b16 v[102:103], v241 offset:55296\n"
  "v_max3_f32 v240, v128, v116, v117\n"
  "v_mfma_f32_16x16x32_bf16 v[192:195], v[160:163], v[104:107], v[192:195]\n"
  "s_waitcnt lgkmcnt(8)\n"
  "v_rcp_f32 v168, v168\n"
  "v_mfma_f32_16x16x32_bf16 v[196:199], v[164:167], v[104:107], v[196:199]\n"
  "s_waitcnt lgkmcnt(6)\n"
  "v_mul_f32 v172, v172, v168\n"
  "v_mul_f32 v173, v173, v168\n"
  "v_cvt_pk_bf16_f32 v172, v172, v173\n"
  "v_mul_f32 v174, v174, v168\n"
  "v_mul_f32 v175, v175, v168\n"
  "v_cvt_pk_bf16_f32 v173, v174, v175\n"
  "v_mul_f32 v176, v176, v168\n"
  "v_mul_f32 v177, v177, v168\n"
  "v_cvt_pk_bf16_f32 v174, v176, v177\n"
  "v_mul_f32 v178, v178, v168\n"
  "v_mul_f32 v179, v179, v168\n"
  "v_cvt_pk_bf16_f32 v175, v178, v179\n"
  "ds_write_b128 v251, v[172:175] offset:27648\n"
  "v_mul_f32 v180, v180, v168\n"
  "v_mul_f32 v181, v181, v168\n"
  "v_cvt_pk_bf16_f32 v180, v180, v181\n"
  "v_mul_f32 v182, v182, v168\n"
  "v_mul_f32 v183, v183, v168\n"
  "v_cvt_pk_bf16_f32 v181, v182, v183\n"
  "ds_write_b64 v247, v[180:181] offset:27648\n"
  "v_rcp_f32 v184, v184\n"
  "v_max3_f32 v240, v240, v118, v119\n"
  "v_mul_f32 v188, v188, v184\n"
  "v_mul_f32 v189, v189, v184\n"
  "v_cvt_pk_bf16_f32 v188, v188, v189\n"
  "v_mul_f32 v190, v190, v184\n"
  "v_mul_f32 v191, v191, v184\n"
  "v_cvt_pk_bf16_f32 v189, v190, v191\n"
  "v_mul_f32 v192, v192, v184\n"
  "v_mul_f32 v193, v193, v184\n"
  "v_cvt_pk_bf16_f32 v190, v192, v193\n"
  "v_mul_f32 v194, v194, v184\n"
  "v_mul_f32 v195, v195, v184\n"
  "v_cvt_pk_bf16_f32 v191, v194, v195\n"
  "ds_write_b128 v251, v[188:191] offset:33792\n"
  "v_mul_f32 v196, v196, v184\n"
  "v_mul_f32 v197, v197, v184\n"
  "v_cvt_pk_bf16_f32 v196, v196, v197\n"
  "v_mul_f32 v198, v198, v184\n"
  "v_mul_f32 v199, v199, v184\n"
  "v_cvt_pk_bf16_f32 v197, v198, v199\n"
  "ds_write_b64 v247, v[196:197] offset:33792\n"
  "v_max3_f32 v240, v240, v120, v121\n"
  "v_max3_f32 v240, v240, v122, v123\n"
  "v_max3_f32 v240, v240, v124, v125\n"
  "v_max3_f32 v240, v240, v126, v127\n"
  "v_max3_f32 v237, v148, v132, v133\n"
  "v_max3_f32 v237, v237, v134, v135\n"
  "v_max3_f32 v237, v237, v136, v137\n"
  "v_max3_f32 v237, v237, v138, v139\n"
  "v_max3_f32 v237, v237, v140, v141\n"
  "v_max3_f32 v237, v237, v142, v143\n"
  "v_add_u32 v255, 2688, v255\n"
  "v_xor_b32 v10, 0xff800000, v8\n"
  "v_permlane16_swap_b32 v240, v237\n"
  "v_max_f32 v240, v240, v237\n"
  "v_mov_b32 v237, v240\n"
  "s_mov_b64 exec, s[70:71]\n"
  "v_mov_b32 v8, 0\n"
  "s_mov_b64 exec, -1\n"
  "v_permlane32_swap_b32 v240, v237\n"
  "s_waitcnt lgkmcnt(0)\n"
  "s_barrier\n"
  "ds_read_b128 v[104:107], v252 offset:27648\n"
  "ds_read_b128 v[168:171], v252 offset:28672\n"
  "ds_read_b128 v[172:175], v252 offset:29696\n"
  "v_max_f32 v240, v240, v237\n"
  "v_mul_f32 v240, 0xbe553b95, v240\n"
  "v_mov_b32 v237, v240\n"
  "v_add_u32 v241, 76800, v251\n"
  "s_nop 0\n"
  "v_permlane16_swap_b32 v240, v237\n"
  "v_fmamk_f32 v120, v120, 0x3e553b95, v240\n"
  "v_exp_f32 v120, v120\n"
  "v_fmamk_f32 v121, v121, 0x3e553b95, v240\n"
  "v_exp_f32 v121, v121\n"
  "v_fmamk_f32 v122, v122, 0x3e553b95, v240\n"
  "v_exp_f32 v122, v122\n"
  "v_cvt_pk_bf16_f32 v120, v120, v121\n"
  "s_waitcnt lgkmcnt(2)\n"
  "v_mfma_f32_16x16x32_bf16 v[204:207], a[216:219], v[104:107], v[204:207]\n"
  "ds_write_b128 v241, v[32:35] offset:0\n"
  "v_mfma_f32_16x16x32_bf16 v[208:211], a[232:235], v[104:107], v[208:211]\n"
  "s_waitcnt lgkmcnt(1)\n"
  "ds_write_b128 v241, v[36:39] offset:4096\n"
  "v_mfma_f32_16x16x32_bf16 v[212:215], a[244:247], v[104:107], v[212:215]\n"
  "v_fmamk_f32 v123, v123, 0x3e553b95, v240\n"
  "v_fmamk_f32 v116, v116, 0x3e553b95, v240\n"
  "v_mfma_f32_16x16x32_bf16 v[204:207], a[220:223], v[168:171], v[204:207]\n"
  "v_exp_f32 v123, v123\n"
  "v_mfma_f32_16x16x32_bf16 v[208:211], a[236:239], v[168:171], v[208:211]\n"
  "ds_read_b128 v[104:107], v252 offset:30720\n"
  "v_cvt_pk_bf16_f32 v121, v122, v123\n"
  "v_mfma_f32_16x16x32_bf16 v[212:215], a[248:251], v[168:171], v[212:215]\n"
  "ds_write_b128 v241, v[200:203] offset:8192\n"
  "v_mfma_f32_16x16x32_bf16 v[204:207], a[224:227], v[172:175], v[204:207]\n"
  "v_exp_f32 v116, v116\n"
  "v_mfma_f32_16x16x32_bf16 v[208:211], a[240:243], v[172:175], v[208:211]\n"
  "ds_write_b128 v241, v[40:43] offset:12288\n"
  "v_mfma_f32_16x16x32_bf16 v[212:215], a[252:255], v[172:175], v[212:215]\n"
  "v_fmamk_f32 v117, v117, 0x3e553b95, v240\n"
  "v_fmamk_f32 v118, v118, 0x3e553b95, v240\n"
  "s_waitcnt lgkmcnt(2)\n"
  "v_mfma_f32_16x16x32_bf16 v[204:207], a[228:231], v[104:107], v[204:207]\n"
  "v_exp_f32 v117, v117\n"
  "v_mfma_f32_16x16x32_bf16 v[208:211], v[4:7], v[104:107], v[208:211]\n"
  "ds_write_b128 v241, v[48:51] offset:16384\n"
  "v_mfma_f32_16x16x32_bf16 v[212:215], v[76:79], v[104:107], v[212:215]\n"
  "v_cvt_pk_bf16_f32 v122, v116, v117\n"
  "v_exp_f32 v118, v118\n"
  "v_fmamk_f32 v119, v119, 0x3e553b95, v240\n"
  "v_exp_f32 v119, v119\n"
  "v_fmamk_f32 v236, v128, 0x3e553b95, v240\n"
  "v_cvt_pk_bf16_f32 v123, v118, v119\n"
  "v_exp_f32 v236, v236\n"
  "v_fmamk_f32 v124, v124, 0x3e553b95, v240\n"
  "v_mfma_f32_16x16x32_bf16 v[168:171], v[16:19], v[120:123], 0\n"
  "ds_write_b128 v241, v[44:47] offset:20480\n"
  "v_mfma_f32_16x16x32_bf16 v[104:107], v[108:111], v[120:123], 0\n"
  "v_exp_f32 v124, v124\n"
  "v_mfma_f32_16x16x32_bf16 v[128:131], v[144:147], v[120:123], 0\n"
  "v_fmamk_f32 v125, v125, 0x3e553b95, v240\n"
  "v_fmamk_f32 v126, v126, 0x3e553b95, v240\n"
  "v_mfma_f32_16x16x32_bf16 v[172:175], v[152:155], v[120:123], 0\n"
  "v_exp_f32 v125, v125\n"
  "v_exp_f32 v126, v126\n"
  "v_cvt_pk_bf16_f32 v124, v124, v125\n"
  "v_fmamk_f32 v127, v127, 0x3e553b95, v240\n"
  "v_exp_f32 v127, v127\n"
  "v_fmamk_f32 v136, v136, 0x3e553b95, v237\n"
  "v_cvt_pk_bf16_f32 v125, v126, v127\n"
  "v_cvt_pk_bf16_f32 v126, v236, 0\n"
  "v_mov_b32 v127, 0\n"
  "v_exp_f32 v136, v136\n"
  "v_fmamk_f32 v137, v137, 0x3e553b95, v237\n"
  "v_mfma_f32_16x16x32_bf16 v[168:171], v[16:19], v[124:127], v[168:171]\n"
  "v_exp_f32 v137, v137\n"
  "v_mfma_f32_16x16x32_bf16 v[104:107], v[156:159], v[124:127], v[104:107]\n"
  "v_cvt_pk_bf16_f32 v136, v136, v137\n"
  "v_fmamk_f32 v138, v138, 0x3e553b95, v237\n"
  "v_mfma_f32_16x16x32_bf16 v[128:131], v[160:163], v[124:127], v[128:131]\n"
  "v_exp_f32 v138, v138\n"
  "v_mfma_f32_16x16x32_bf16 v[172:175], v[164:167], v[124:127], v[172:175]\n"
  "v_fmamk_f32 v139, v139, 0x3e553b95, v237\n"
  "v_exp_f32 v139, v139\n"
  "v_fmamk_f32 v132, v132, 0x3e553b95, v237\n"
  "v_cvt_pk_bf16_f32 v137, v138, v139\n"
  "v_exp_f32 v132, v132\n"
  "v_fmamk_f32 v133, v133, 0x3e553b95, v237\n"
  "v_exp_f32 v133, v133\n"
  "v_fmamk_f32 v134, v134, 0x3e553b95, v237\n"
  "v_cvt_pk_bf16_f32 v138, v132, v133\n"
  "v_exp_f32 v134, v134\n"
  "v_fmamk_f32 v135, v135, 0x3e553b95, v237\n"
  "v_exp_f32 v135, v135\n"
  "v_fmamk_f32 v240, v148, 0x3e553b95, v237\n"
  "v_cvt_pk_bf16_f32 v139, v134, v135\n"
  "v_exp_f32 v240, v240\n"
  "v_fmamk_f32 v140, v140, 0x3e553b95, v237\n"
  "v_mfma_f32_16x16x32_bf16 v[148:151], v[16:19], v[136:139], 0\n"
  "v_exp_f32 v140, v140\n"
  "v_mfma_f32_16x16x32_bf16 v[176:179], v[108:111], v[136:139], 0\n"
  "ds_read_b128 v[116:119], v252 offset:31744\n"
  "v_fmamk_f32 v141, v141, 0x3e553b95, v237\n"
  "v_mfma_f32_16x16x32_bf16 v[180:183], v[144:147], v[136:139], 0\n"
  "v_exp_f32 v141, v141\n"
  "v_mfma_f32_16x16x32_bf16 v[184:187], v[152:155], v[136:139], 0\n"
  "v_cvt_pk_bf16_f32 v140, v140, v141\n"
  "v_fmamk_f32 v142, v142, 0x3e553b95, v237\n"
  "v_exp_f32 v142, v142\n"
  "v_fmamk_f32 v143, v143, 0x3e553b95, v237\n"
  "v_exp_f32 v143, v143\n"
  "s_barrier\n"
  "ds_read_u16 v237, v253 offset:2056\n"
  "s_waitcnt lgkmcnt(1)\n"
  "v_mfma_f32_16x16x32_bf16 v[204:207], v[80:83], v[116:119], v[204:207]\n"
  "v_cvt_pk_bf16_f32 v141, v142, v143\n"
  "v_cvt_pk_bf16_f32 v142, v240, 0\n"
  "v_mfma_f32_16x16x32_bf16 v[208:211], v[84:87], v[116:119], v[208:211]\n"
  "v_mov_b32 v143, 0\n"
  "v_mfma_f32_16x16x32_bf16 v[212:215], v[88:91], v[116:119], v[212:215]\n"
  "v_rcp_f32 v168, v168\n"
  "v_mfma_f32_16x16x32_bf16 v[148:151], v[16:19], v[140:143], v[148:151]\n"
  "ds_read_b128 v[108:111], v252 offset:32768\n"
  "v_mul_f32 v104, v104, v168\n"
  "v_mfma_f32_16x16x32_bf16 v[176:179], v[156:159], v[140:143], v[176:179]\n"
  "s_waitcnt lgkmcnt(1)\n"
  "v_add_u32 v240, v237, v250\n"
  "v_add_u32 v236, v237, v249\n"
  "v_mfma_f32_16x16x32_bf16 v[180:183], v[160:163], v[140:143], v[180:183]\n"
  "v_add_u32 v233, v237, v248\n"
  "v_mul_f32 v105, v105, v168\n"
  "v_mfma_f32_16x16x32_bf16 v[184:187], v[164:167], v[140:143], v[184:187]\n"
  "ds_read_b128 v[120:123], v240 offset:0\n"
  "ds_read_b128 v[124:127], v252 offset:33792\n"
  "v_cvt_pk_bf16_f32 v104, v104, v105\n"
  "v_mul_f32 v106, v106, v168\n"
  "v_mul_f32 v107, v107, v168\n"
  "v_cvt_pk_bf16_f32 v105, v106, v107\n"
  "v_mul_f32 v128, v128, v168\n"
  "v_mul_f32 v129, v129, v168\n"
  "s_waitcnt lgkmcnt(2)\n"
  "v_mfma_f32_16x16x32_bf16 v[204:207], v[92:95], v[108:111], v[204:207]\n"
  "ds_read_b128 v[132:135], v236 offset:0\n"
  "ds_read_b128 v[136:139], v236 offset:3072\n"
  "v_mfma_f32_16x16x32_bf16 v[208:211], v[96:99], v[108:111], v[208:211]\n"
  "ds_read_u16 v237, v253 offset:2058\n"
  "v_cvt_pk_bf16_f32 v106, v128, v129\n"
  "v_mfma_f32_16x16x32_bf16 v[212:215], v[100:103], v[108:111], v[212:215]\n"
  "ds_read_b128 v[140:143], v233 offset:0\n"
  "ds_read_b128 v[144:147], v233 offset:3072\n"
  "s_waitcnt lgkmcnt(6)\n"
  "v_mfma_f32_16x16x32_bf16 v[116:119], v[24:27], v[120:123], 0\n"
  "s_waitcnt lgkmcnt(5)\n"
  "ds_read_b128 v[108:111], v240 offset:128\n"
  "v_cvt_pk_bf16_f32 v204, v204, v205\n"
  "v_mfma_f32_16x16x32_bf16 v[216:219], a[216:219], v[124:127], v[216:219]\n"
  "ds_read_b128 v[188:191], v236 offset:128\n"
  "v_cvt_pk_bf16_f32 v205, v206, v207\n"
  "v_mfma_f32_16x16x32_bf16 v[220:223], a[232:235], v[124:127], v[220:223]\n"
  "s_waitcnt lgkmcnt(6)\n"
  "ds_write_b64 v244, v[204:205] offset:0\n"
  "s_waitcnt lgkmcnt(6)\n"
  "v_smfmac_f32_16x16x64_bf16 v[116:119], v[20:23], v[132:139], v9\n"
  "s_waitcnt lgkmcnt(5)\n"
  "ds_read_b128 v[192:195], v236 offset:3200\n"
  "v_mfma_f32_16x16x32_bf16 v[224:227], a[244:247], v[124:127], v[224:227]\n"
  "s_waitcnt lgkmcnt(5)\n"
  "v_cvt_pk_bf16_f32 v208, v208, v209\n"
  "v_cvt_pk_bf16_f32 v209, v210, v211\n"
  "v_cvt_pk_bf16_f32 v212, v212, v213\n"
  "s_waitcnt lgkmcnt(4)\n"
  "v_smfmac_f32_16x16x64_bf16 v[116:119], v[28:31], v[140:147], v9\n"
  "ds_write_b64 v244, v[208:209] offset:128\n"
  "s_waitcnt lgkmcnt(4)\n"
  "v_mfma_f32_16x16x32_bf16 v[120:123], v[200:203], v[108:111], 0\n"
  "s_waitcnt lgkmcnt(3)\n"
  "ds_read_b128 v[124:127], v252 offset:34816\n"
  "ds_read_b128 v[132:135], v233 offset:128\n"
  "v_cvt_pk_bf16_f32 v213, v214, v215\n"
  "ds_write_b64 v244, v[212:213] offset:256\n"
  "v_add_u32 v232, v237, v250\n"
  "v_add_u32 v15, v237, v249\n"
  "v_add_u32 v215, v237, v248\n"
  "s_waitcnt lgkmcnt(4)\n"
  "v_smfmac_f32_16x16x64_bf16 v[120:123], v[32:35], v[188:195], v9\n"
  "ds_read_b128 v[136:139], v233 offset:3200\n"
  "ds_read_b128 v[108:111], v240 offset:256\n"
  "v_add_f32 v237, v116, v117\n"
  "v_mul_f32 v214, v116, v116\n"
  "v_fmac_f32 v214, v117, v117\n"
  "v_add_f32 v237, v118, v237\n"
  "v_fmac_f32 v214, v118, v118\n"
  "s_waitcnt lgkmcnt(4)\n"
  "v_mfma_f32_16x16x32_bf16 v[216:219], a[220:223], v[124:127], v[216:219]\n"
  "ds_read_b128 v[140:143], v236 offset:256\n"
  "ds_read_b128 v[144:147], v236 offset:3328\n"
  "v_mfma_f32_16x16x32_bf16 v[220:223], a[236:239], v[124:127], v[220:223]\n"
  "s_waitcnt lgkmcnt(5)\n"
  "ds_read_b128 v[188:191], v233 offset:256\n"
  "v_add_f32 v237, v119, v237\n"
  "v_mfma_f32_16x16x32_bf16 v[224:227], a[248:251], v[124:127], v[224:227]\n"
  "ds_read_b128 v[192:195], v233 offset:3328\n"
  "v_fmac_f32 v214, v119, v119\n"
  "v_mul_f32 v130, v130, v168\n"
  "v_mul_f32 v131, v131, v168\n"
  "v_cvt_pk_bf16_f32 v107, v130, v131\n"
  "s_waitcnt lgkmcnt(5)\n"
  "v_smfmac_f32_16x16x64_bf16 v[120:123], v[36:39], v[132:139], v9\n"
  "ds_read_b128 v[152:155], v232 offset:0\n"
  "ds_read_b128 v[196:199], v252 offset:35840\n"
  "s_waitcnt lgkmcnt(6)\n"
  "v_mfma_f32_16x16x32_bf16 v[124:127], v[44:47], v[108:111], 0\n"
  "s_waitcnt lgkmcnt(5)\n"
  "ds_read_b128 v[132:135], v15 offset:0\n"
  "v_mul_f32 v172, v172, v168\n"
  "v_mul_f32 v173, v173, v168\n"
  "v_add_f32 v237, v120, v237\n"
  "s_waitcnt lgkmcnt(5)\n"
  "v_smfmac_f32_16x16x64_bf16 v[124:127], v[40:43], v[140:147], v9\n"
  "ds_read_b128 v[136:139], v15 offset:3072\n"
  "ds_read_b128 v[204:207], v215 offset:0\n"
  "v_fmac_f32 v214, v120, v120\n"
  "v_add_f32 v237, v121, v237\n"
  "v_fmac_f32 v214, v121, v121\n"
  "s_waitcnt lgkmcnt(5)\n"
  "v_smfmac_f32_16x16x64_bf16 v[124:127], v[48:51], v[188:195], v9\n"
  "ds_read_b128 v[208:211], v215 offset:3072\n"
  "ds_read_b128 v[108:111], v232 offset:128\n"
  "s_waitcnt lgkmcnt(6)\n"
  "v_mfma_f32_16x16x32_bf16 v[128:131], v[24:27], v[152:155], 0\n"
  "s_waitcnt lgkmcnt(5)\n"
  "ds_read_u16 v240, v253 offset:2060\n"
  "ds_write_b128 v251, v[104:107] offset:39936\n"
  "v_mfma_f32_16x16x32_bf16 v[216:219], a[224:227], v[196:199], v[216:219]\n"
  "s_waitcnt lgkmcnt(5)\n"
  "v_add_f32 v237, v122, v237\n"
  "v_fmac_f32 v214, v122, v122\n"
  "v_smfmac_f32_16x16x64_bf16 v[128:131], v[20:23], v[132:139], v9\n"
  "s_waitcnt lgkmcnt(4)\n"
  "ds_read_b128 v[140:143], v15 offset:128\n"
  "v_mfma_f32_16x16x32_bf16 v[220:223], a[240:243], v[196:199], v[220:223]\n"
  "s_waitcnt lgkmcnt(4)\n"
  "v_add_f32 v237, v123, v237\n"
  "v_fmac_f32 v214, v123, v123\n"
  "v_smfmac_f32_16x16x64_bf16 v[128:131], v[28:31], v[204:211], v9\n"
  "s_waitcnt lgkmcnt(3)\n"
  "ds_read_b128 v[144:147], v15 offset:3200\n"
  "ds_read_b128 v[104:107], v252 offset:36864\n"
  "v_mfma_f32_16x16x32_bf16 v[132:135], v[200:203], v[108:111], 0\n"
  "s_waitcnt lgkmcnt(4)\n"
  "v_add_u32 v236, v240, v250\n"
  "v_add_u32 v233, v240, v249\n"
  "v_mfma_f32_16x16x32_bf16 v[224:227], a[252:255], v[196:199], v[224:227]\n"
  "ds_read_b128 v[188:191], v215 offset:128\n"
  "ds_read_b128 v[192:195], v215 offset:3200\n"
  "ds_read_b128 v[108:111], v232 offset:256\n"
  "v_add_u32 v14, v240, v248\n"
  "v_add_f32 v237, v124, v237\n"
  "v_fmac_f32 v214, v124, v124\n"
  "v_add_f32 v237, v125, v237\n"
  "v_fmac_f32 v214, v125, v125\n"
  "v_add_f32 v237, v126, v237\n"
  "v_fmac_f32 v214, v126, v126\n"
  "v_add_f32 v237, v127, v237\n"
  "v_fmac_f32 v214, v127, v127\n"
  "s_waitcnt lgkmcnt(4)\n"
  "v_smfmac_f32_16x16x64_bf16 v[132:135], v[32:35], v[140:147], v9\n"
  "ds_read_b128 v[204:207], v15 offset:256\n"
  "ds_read_b128 v[208:211], v15 offset:3328\n"
  "s_waitcnt lgkmcnt(5)\n"
  "v_mfma_f32_16x16x32_bf16 v[216:219], a[228:231], v[104:107], v[216:219]\n"
  "s_waitcnt lgkmcnt(4)\n"
  "ds_read_b128 v[140:143], v215 offset:256\n"
  "ds_read_b128 v[144:147], v215 offset:3328\n"
  "v_mfma_f32_16x16x32_bf16 v[220:223], v[4:7], v[104:107], v[220:223]\n"
  "s_waitcnt lgkmcnt(5)\n"
  "v_permlane16_swap_b32 v237, v214\n"
  "v_smfmac_f32_16x16x64_bf16 v[132:135], v[36:39], v[188:195], v9\n"
  "s_waitcnt lgkmcnt(4)\n"
  "ds_read_b128 v[152:155], v236 offset:0\n"
  "ds_read_b128 v[188:191], v252 offset:37888\n"
  "v_mfma_f32_16x16x32_bf16 v[136:139], v[44:47], v[108:111], 0\n"
  "v_add_f32 v237, v237, v214\n"
  "v_add_f32 v240, v128, v129\n"
  "v_mfma_f32_16x16x32_bf16 v[224:227], v[76:79], v[104:107], v[224:227]\n"
  "s_waitcnt lgkmcnt(5)\n"
  "ds_read_b128 v[192:195], v233 offset:0\n"
  "ds_read_b128 v[196:199], v233 offset:3072\n"
  "v_mul_f32 v232, v128, v128\n"
  "s_waitcnt lgkmcnt(6)\n"
  "v_smfmac_f32_16x16x64_bf16 v[136:139], v[40:43], v[204:211], v9\n"
  "ds_read_b128 v[204:207], v14 offset:0\n"
  "v_fmac_f32 v232, v129, v129\n"
  "v_add_f32 v240, v130, v240\n"
  "v_fmac_f32 v232, v130, v130\n"
  "v_add_f32 v240, v131, v240\n"
  "s_waitcnt lgkmcnt(5)\n"
  "v_smfmac_f32_16x16x64_bf16 v[136:139], v[48:51], v[140:147], v9\n"
  "ds_read_b128 v[208:211], v14 offset:3072\n"
  "ds_read_b128 v[212:215], v236 offset:128\n"
  "s_waitcnt lgkmcnt(6)\n"
  "v_mfma_f32_16x16x32_bf16 v[104:107], v[24:27], v[152:155], 0\n"
  "s_waitcnt lgkmcnt(5)\n"
  "v_fmac_f32 v232, v131, v131\n"
  "v_add_f32 v240, v132, v240\n"
  "v_mfma_f32_16x16x32_bf16 v[216:219], v[80:83], v[188:191], v[216:219]\n"
  "s_waitcnt lgkmcnt(4)\n"
  "ds_read_b128 v[140:143], v233 offset:128\n"
  "ds_read_b128 v[144:147], v233 offset:3200\n"
  "v_mfma_f32_16x16x32_bf16 v[220:223], v[84:87], v[188:191], v[220:223]\n"
  "s_waitcnt lgkmcnt(5)\n"
  "ds_read_b128 v[152:155], v252 offset:38912\n"
  "v_fmac_f32 v232, v132, v132\n"
  "v_smfmac_f32_16x16x64_bf16 v[104:107], v[20:23], v[192:199], v9\n"
  "s_waitcnt lgkmcnt(5)\n"
  "ds_read_b128 v[192:195], v14 offset:128\n"
  "v_add_f32 v240, v133, v240\n"
  "v_mfma_f32_16x16x32_bf16 v[224:227], v[88:91], v[188:191], v[224:227]\n"
  "s_waitcnt lgkmcnt(5)\n"
  "ds_read_b128 v[196:199], v14 offset:3200\n"
  "v_fmac_f32 v232, v133, v133\n"
  "v_smfmac_f32_16x16x64_bf16 v[104:107], v[28:31], v[204:211], v9\n"
  "s_waitcnt lgkmcnt(5)\n"
  "ds_read_b128 v[188:191], v236 offset:256\n"
  "v_add_f32 v240, v134, v240\n"
  "v_mfma_f32_16x16x32_bf16 v[108:111], v[200:203], v[212:215], 0\n"
  "s_waitcnt lgkmcnt(5)\n"
  "ds_read_b128 v[204:207], v233 offset:256\n"
  "v_fmac_f32 v232, v134, v134\n"
  "v_add_f32 v240, v135, v240\n"
  "v_fmac_f32 v232, v135, v135\n"
  "s_waitcnt lgkmcnt(5)\n"
  "v_smfmac_f32_16x16x64_bf16 v[108:111], v[32:35], v[140:147], v9\n"
  "ds_read_b128 v[208:211], v233 offset:3328\n"
  "v_add_f32 v240, v136, v240\n"
  "s_waitcnt lgkmcnt(5)\n"
  "v_mfma_f32_16x16x32_bf16 v[216:219], v[92:95], v[152:155], v[216:219]\n"
  "s_waitcnt lgkmcnt(4)\n"
  "ds_read_b128 v[140:143], v14 offset:256\n"
  "ds_read_b128 v[144:147], v14 offset:3328\n"
  "v_mfma_f32_16x16x32_bf16 v[220:223], v[96:99], v[152:155], v[220:223]\n"
  "s_waitcnt lgkmcnt(5)\n"
  "v_fmac_f32 v232, v136, v136\n"
  "v_add_f32 v240, v137, v240\n"
  "v_smfmac_f32_16x16x64_bf16 v[108:111], v[36:39], v[192:199], v9\n"
  "s_waitcnt lgkmcnt(4)\n"
  "s_nop 0\n"
  "v_cvt_pk_bf16_f32 v216, v216, v217\n"
  "v_mfma_f32_16x16x32_bf16 v[32:35], v[44:47], v[188:191], 0\n"
  "s_waitcnt lgkmcnt(3)\n"
  "v_cvt_pk_bf16_f32 v217, v218, v219\n"
  "v_cvt_pk_bf16_f32 v220, v220, v221\n"
  "v_mfma_f32_16x16x32_bf16 v[224:227], v[100:103], v[152:155], v[224:227]\n"
  "s_waitcnt lgkmcnt(2)\n"
  "ds_write_b64 v244, v[216:217] offset:6144\n"
  "v_smfmac_f32_16x16x64_bf16 v[32:35], v[40:43], v[204:211], v9\n"
  "s_waitcnt lgkmcnt(2)\n"
  "s_nop 0\n"
  "v_cvt_pk_bf16_f32 v221, v222, v223\n"
  "v_fmac_f32 v232, v137, v137\n"
  "s_waitcnt lgkmcnt(1)\n"
  "v_smfmac_f32_16x16x64_bf16 v[32:35], v[48:51], v[140:147], v9\n"
  "ds_write_b64 v244, v[220:221] offset:6272\n"
  "v_cvt_pk_bf16_f32 v224, v224, v225\n"
  "v_cvt_pk_bf16_f32 v225, v226, v227\n"
  "ds_write_b64 v244, v[224:225] offset:6400\n"
  "v_add_f32 v240, v138, v240\n"
  "v_fmac_f32 v232, v138, v138\n"
  "v_add_f32 v240, v139, v240\n"
  "v_fmac_f32 v232, v139, v139\n"
  "v_add_f32 v236, v104, v105\n"
  "v_mul_f32 v233, v104, v104\n"
  "v_permlane16_swap_b32 v240, v232\n"
  "v_add_f32 v240, v240, v232\n"
  "v_fmac_f32 v233, v105, v105\n"
  "v_add_f32 v236, v106, v236\n"
  "v_permlane32_swap_b32 v237, v240\n"
  "v_add_f32 v237, v237, v240\n"
  "ds_write_b32 v246, v237 offset:0\n"
  "v_fmac_f32 v233, v106, v106\n"
  "v_add_f32 v236, v107, v236\n"
  "v_fmac_f32 v233, v107, v107\n"
  "v_add_f32 v236, v108, v236\n"
  "v_fmac_f32 v233, v108, v108\n"
  "v_add_f32 v236, v109, v236\n"
  "v_fmac_f32 v233, v109, v109\n"
  "v_add_f32 v236, v110, v236\n"
  "v_fmac_f32 v233, v110, v110\n"
  "v_add_f32 v236, v111, v236\n"
  "v_fmac_f32 v233, v111, v111\n"
  "v_add_f32 v236, v32, v236\n"
  "v_fmac_f32 v233, v32, v32\n"
  "v_add_f32 v236, v33, v236\n"
  "v_fmac_f32 v233, v33, v33\n"
  "v_add_f32 v236, v34, v236\n"
  "v_fmac_f32 v233, v34, v34\n"
  "v_add_f32 v236, v35, v236\n"
  "v_fmac_f32 v233, v35, v35\n"
  "v_cvt_pk_bf16_f32 v172, v172, v173\n"
  "v_mul_f32 v174, v174, v168\n"
  "v_permlane16_swap_b32 v236, v233\n"
  "v_add_f32 v236, v236, v233\n"
  "v_mov_b32 v240, v236\n"
  "v_mul_f32 v175, v175, v168\n"
  "v_cvt_pk_bf16_f32 v173, v174, v175\n"
  "v_permlane32_swap_b32 v236, v240\n"
  "v_add_f32 v236, v236, v240\n"
  "ds_write_b32 v246, v236 offset:1024\n"
  "ds_write_b64 v247, v[172:173] offset:39936\n"
  "v_rcp_f32 v148, v148\n"
  "s_nop 0\n"
  "v_mul_f32 v176, v176, v148\n"
  "v_mul_f32 v177, v177, v148\n"
  "v_cvt_pk_bf16_f32 v176, v176, v177\n"
  "v_mul_f32 v178, v178, v148\n"
  "v_mul_f32 v179, v179, v148\n"
  "v_cvt_pk_bf16_f32 v177, v178, v179\n"
  "v_mul_f32 v180, v180, v148\n"
  "v_mul_f32 v181, v181, v148\n"
  "v_cvt_pk_bf16_f32 v178, v180, v181\n"
  "v_mul_f32 v182, v182, v148\n"
  "v_mul_f32 v183, v183, v148\n"
  "v_cvt_pk_bf16_f32 v179, v182, v183\n"
  "v_mul_f32 v184, v184, v148\n"
  "v_mul_f32 v185, v185, v148\n"
  "s_waitcnt lgkmcnt(1)\n"
  "s_barrier\n"
  "ds_read_b128 v[36:39], v253 offset:0\n"
  "ds_read_b128 v[40:43], v253 offset:256\n"
  "ds_read_b128 v[44:47], v253 offset:512\n"
  "s_cmp_eq_u32 s88, 196\n"
  "s_cselect_b32 s84, 128, -1\n"
  "v_cmp_eq_u32 vcc, s84, v254\n"
  "v_bfrev_b32 v240, 1\n"
  "v_add_u32 v237, 64, v254\n"
  "v_cndmask_b32_e64 v236, v255, v240, vcc\n"
  "v_bitop3_b32 v233, v236, v237, s91 bitop3:0x78\n"
  "s_add_u32 m0, s81, 31232\n"
  "v_add_u32 v255, 80, v254\n"
  "v_bitop3_b32 v240, v236, v255, s91 bitop3:0x78\n"
  "v_add_u32 v237, 96, v254\n"
  "v_bitop3_b32 v232, v236, v237, s91 bitop3:0x78\n"
  "v_add_u32 v255, 112, v254\n"
  "v_bitop3_b32 v227, v236, v255, s91 bitop3:0x78\n"
  "v_add_u32 v237, 0, v254\n"
  "v_bitop3_b32 v226, v236, v237, s91 bitop3:0x78\n"
  "v_add_u32 v255, 16, v254\n"
  "s_waitcnt lgkmcnt(2)\n"
  "v_add_f32 v36, v36, v37\n"
  "v_add_f32 v38, v38, v39\n"
  "v_add_f32 v36, v36, v38\n"
  "s_waitcnt lgkmcnt(1)\n"
  "v_add_f32 v40, v40, v41\n"
  "v_add_f32 v42, v42, v43\n"
  "v_add_f32 v40, v40, v42\n"
  "v_mul_f32 v36, 0xbbaaaaab, v36\n"
  "v_mul_f32 v40, 0x3baaaaab, v40\n"
  "v_fma_f32 v40, -v36, v36, v40\n"
  "v_add_f32_e64 v40, abs(v40), s43\n"
  "v_rsq_f32 v40, v40\n"
  "v_bitop3_b32 v237, v236, v255, s91 bitop3:0x78\n"
  "v_mul_f32 v36, v36, v40\n"
  "v_fma_f32 v37, v116, v40, v36\n"
  "v_fma_f32 v38, v117, v40, v36\n"
  "v_cvt_pk_bf16_f32 v48, v37, v38\n"
  "v_fma_f32 v41, v118, v40, v36\n"
  "v_fma_f32 v42, v119, v40, v36\n"
  "v_cvt_pk_bf16_f32 v49, v41, v42\n"
  "v_fma_f32 v37, v120, v40, v36\n"
  "v_fma_f32 v38, v121, v40, v36\n"
  "v_cvt_pk_bf16_f32 v50, v37, v38\n"
  "v_fma_f32 v41, v122, v40, v36\n"
  "v_fma_f32 v42, v123, v40, v36\n"
  "v_cvt_pk_bf16_f32 v51, v41, v42\n"
  "ds_write_b128 v251, v[48:51] offset:0\n"
  "s_mov_b64 exec, s[54:55]\n"
  "buffer_load_dwordx4 v233, s[20:23], s44 offen nt lds\n"
  "s_mov_b64 exec, -1\n"
  "ds_read_b128 v[140:143], v253 offset:768\n"
  "v_fma_f32 v37, v124, v40, v36\n"
  "v_fma_f32 v38, v125, v40, v36\n"
  "v_cvt_pk_bf16_f32 v224, v37, v38\n"
  "v_fma_f32 v41, v126, v40, v36\n"
  "v_fma_f32 v42, v127, v40, v36\n"
  "v_cvt_pk_bf16_f32 v225, v41, v42\n"
  "s_add_u32 m0, s81, 31232\n"
  "ds_write_b64 v247, v[224:225] offset:0\n"
  "s_mov_b64 exec, s[54:55]\n"
  "buffer_load_dwordx4 v240, s[20:23], s45 offen offset:3456 nt lds\n"
  "s_mov_b64 exec, -1\n"
  "ds_read_b128 v[36:39], v253 offset:1024\n"
  "ds_read_b128 v[40:43], v253 offset:1280\n"
  "s_add_u32 m0, s81, 38144\n"
  "v_cvt_pk_bf16_f32 v184, v184, v185\n"
  "v_mul_f32 v186, v186, v148\n"
  "v_mul_f32 v187, v187, v148\n"
  "v_cvt_pk_bf16_f32 v185, v186, v187\n"
  "s_waitcnt lgkmcnt(5)\n"
  "v_add_f32 v44, v44, v45\n"
  "v_add_f32 v46, v46, v47\n"
  "v_add_f32 v44, v44, v46\n"
  "s_waitcnt lgkmcnt(3)\n"
  "v_add_f32 v140, v140, v141\n"
  "v_add_f32 v142, v142, v143\n"
  "v_add_f32 v140, v140, v142\n"
  "v_mul_f32 v44, 0xbbaaaaab, v44\n"
  "v_mul_f32 v140, 0x3baaaaab, v140\n"
  "s_waitcnt lgkmcnt(2)\n"
  "s_barrier\n"
  "ds_read_b128 v[48:51], v252 offset:39936\n"
  "ds_read_b128 v[144:147], v252 offset:40960\n"
  "ds_read_b128 v[148:151], v252 offset:41984\n"
  "s_mov_b64 exec, s[54:55]\n"
  "buffer_load_dwordx4 v232, s[20:23], s46 offen nt lds\n"
  "s_mov_b64 exec, -1\n"
  "s_mov_b64 exec, s[62:63]\n"
  "ds_write_b128 v251, v[176:179] offset:46080\n"
  "ds_write_b64 v247, v[184:185] offset:46080\n"
  "s_mov_b64 exec, -1\n"
  "s_add_u32 m0, s81, 38144\n"
  "v_fma_f32 v140, -v44, v44, v140\n"
  "v_add_f32_e64 v140, abs(v140), s43\n"
  "s_waitcnt lgkmcnt(4)\n"
  "v_mfma_f32_16x16x32_bf16 v[52:55], a[216:219], v[48:51], v[52:55]\n"
  "s_mov_b64 exec, s[54:55]\n"
  "buffer_load_dwordx4 v227, s[20:23], s47 offen offset:3456 nt lds\n"
  "s_mov_b64 exec, -1\n"
  "v_mfma_f32_16x16x32_bf16 v[56:59], a[232:235], v[48:51], v[56:59]\n"
  "s_add_u32 m0, s81, 45056\n"
  "v_rsq_f32 v140, v140\n"
  "v_mfma_f32_16x16x32_bf16 v[60:63], a[244:247], v[48:51], v[60:63]\n"
  "s_waitcnt lgkmcnt(2)\n"
  "ds_read_b128 v[152:155], v252 offset:43008\n"
  "s_mov_b64 exec, s[54:55]\n"
  "buffer_load_dwordx4 v226, s[20:23], s48 offen nt lds\n"
  "s_mov_b64 exec, -1\n"
  "v_mfma_f32_16x16x32_bf16 v[52:55], a[220:223], v[144:147], v[52:55]\n"
  "s_add_u32 m0, s81, 45056\n"
  "v_mul_f32 v44, v44, v140\n"
  "v_fma_f32 v45, v128, v140, v44\n"
  "v_mfma_f32_16x16x32_bf16 v[56:59], a[236:239], v[144:147], v[56:59]\n"
  "v_fma_f32 v46, v129, v140, v44\n"
  "v_cvt_pk_bf16_f32 v48, v45, v46\n"
  "v_mfma_f32_16x16x32_bf16 v[60:63], a[248:251], v[144:147], v[60:63]\n"
  "ds_read_b128 v[168:171], v252 offset:44032\n"
  "s_mov_b64 exec, s[54:55]\n"
  "buffer_load_dwordx4 v237, s[20:23], s49 offen offset:3456 nt lds\n"
  "s_mov_b64 exec, -1\n"
  "v_mfma_f32_16x16x32_bf16 v[52:55], a[224:227], v[148:151], v[52:55]\n"
  "s_add_u32 m0, s81, 51968\n"
  "v_fma_f32 v141, v130, v140, v44\n"
  "v_fma_f32 v142, v131, v140, v44\n"
  "v_mfma_f32_16x16x32_bf16 v[56:59], a[240:243], v[148:151], v[56:59]\n"
  "s_waitcnt lgkmcnt(1)\n"
  "v_cvt_pk_bf16_f32 v49, v141, v142\n"
  "v_fma_f32 v45, v132, v140, v44\n"
  "v_mfma_f32_16x16x32_bf16 v[60:63], a[252:255], v[148:151], v[60:63]\n"
  "ds_read_b128 v[144:147], v252 offset:45056\n"
  "v_fma_f32 v46, v133, v140, v44\n"
  "v_mfma_f32_16x16x32_bf16 v[52:55], a[228:231], v[152:155], v[52:55]\n"
  "v_cvt_pk_bf16_f32 v50, v45, v46\n"
  "v_fma_f32 v141, v134, v140, v44\n"
  "v_mfma_f32_16x16x32_bf16 v[56:59], v[4:7], v[152:155], v[56:59]\n"
  "s_waitcnt lgkmcnt(1)\n"
  "v_fma_f32 v142, v135, v140, v44\n"
  "v_cvt_pk_bf16_f32 v51, v141, v142\n"
  "v_mfma_f32_16x16x32_bf16 v[60:63], v[76:79], v[152:155], v[60:63]\n"
  "ds_read_b128 v[148:151], v252 offset:0\n"
  "ds_write_b128 v251, v[48:51] offset:6144\n"
  "v_mfma_f32_16x16x32_bf16 v[52:55], v[80:83], v[168:171], v[52:55]\n"
  "s_waitcnt lgkmcnt(2)\n"
  "v_fma_f32 v45, v136, v140, v44\n"
  "v_fma_f32 v46, v137, v140, v44\n"
  "v_mfma_f32_16x16x32_bf16 v[56:59], v[84:87], v[168:171], v[56:59]\n"
  "v_cvt_pk_bf16_f32 v232, v45, v46\n"
  "v_fma_f32 v141, v138, v140, v44\n"
  "v_mfma_f32_16x16x32_bf16 v[60:63], v[88:91], v[168:171], v[60:63]\n"
  "v_fma_f32 v142, v139, v140, v44\n"
  "v_cvt_pk_bf16_f32 v233, v141, v142\n"
  "v_mfma_f32_16x16x32_bf16 v[52:55], v[92:95], v[144:147], v[52:55]\n"
  "s_waitcnt lgkmcnt(1)\n"
  "ds_write_b64 v247, v[232:233] offset:6144\n"
  "v_mfma_f32_16x16x32_bf16 v[56:59], v[96:99], v[144:147], v[56:59]\n"
  "v_add_f32 v36, v36, v37\n"
  "v_add_f32 v38, v38, v39\n"
  "v_mfma_f32_16x16x32_bf16 v[60:63], v[100:103], v[144:147], v[60:63]\n"
  "ds_read_b128 v[44:47], v252 offset:1024\n"
  "v_add_f32 v36, v36, v38\n"
  "v_mfma_f32_16x16x32_bf16 v[140:143], a[72:75], v[148:151], 0\n"
  "v_cvt_pk_bf16_f32 v52, v52, v53\n"
  "v_cvt_pk_bf16_f32 v53, v54, v55\n"
  "v_mfma_f32_16x16x32_bf16 v[48:51], a[96:99], v[148:151], 0\n"
  "ds_write_b64 v244, v[52:53] offset:12288\n"
  "v_mfma_f32_16x16x32_bf16 v[144:147], a[120:123], v[148:151], 0\n"
  "v_cvt_pk_bf16_f32 v56, v56, v57\n"
  "v_cvt_pk_bf16_f32 v57, v58, v59\n"
  "v_mfma_f32_16x16x32_bf16 v[52:55], a[0:3], v[148:151], 0\n"
  "ds_write_b64 v244, v[56:57] offset:12416\n"
  "v_mfma_f32_16x16x32_bf16 v[152:155], a[24:27], v[148:151], 0\n"
  "s_waitcnt lgkmcnt(2)\n"
  "v_cvt_pk_bf16_f32 v60, v60, v61\n"
  "v_cvt_pk_bf16_f32 v61, v62, v63\n"
  "v_mfma_f32_16x16x32_bf16 v[56:59], a[48:51], v[148:151], 0\n"
  "ds_write_b64 v244, v[60:61] offset:12544\n"
  "v_mfma_f32_16x16x32_bf16 v[140:143], a[76:79], v[44:47], v[140:143]\n"
  "v_add_f32 v40, v40, v41\n"
  "v_add_f32 v42, v42, v43\n"
  "v_mfma_f32_16x16x32_bf16 v[48:51], a[100:103], v[44:47], v[48:51]\n"
  "ds_read_b128 v[60:63], v252 offset:2048\n"
  "v_add_f32 v40, v40, v42\n"
  "v_mfma_f32_16x16x32_bf16 v[144:147], a[124:127], v[44:47], v[144:147]\n"
  "v_mul_f32 v36, 0xbbaaaaab, v36\n"
  "v_mul_f32 v40, 0x3baaaaab, v40\n"
  "v_mfma_f32_16x16x32_bf16 v[52:55], a[4:7], v[44:47], v[52:55]\n"
  "v_fma_f32 v40, -v36, v36, v40\n"
  "v_add_f32_e64 v40, abs(v40), s43\n"
  "v_mfma_f32_16x16x32_bf16 v[152:155], a[28:31], v[44:47], v[152:155]\n"
  "v_rsq_f32 v40, v40\n"
  "v_mfma_f32_16x16x32_bf16 v[56:59], a[52:55], v[44:47], v[56:59]\n"
  "ds_read_b128 v[148:151], v252 offset:3072\n"
  "v_mul_f32 v36, v36, v40\n"
  "v_fma_f32 v37, v104, v40, v36\n"
  "v_fma_f32 v38, v105, v40, v36\n"
  "s_waitcnt lgkmcnt(1)\n"
  "v_mfma_f32_16x16x32_bf16 v[140:143], a[80:83], v[60:63], v[140:143]\n"
  "v_cvt_pk_bf16_f32 v44, v37, v38\n"
  "v_fma_f32 v41, v106, v40, v36\n"
  "v_mfma_f32_16x16x32_bf16 v[48:51], a[104:107], v[60:63], v[48:51]\n"
  "v_fma_f32 v42, v107, v40, v36\n"
  "v_cvt_pk_bf16_f32 v45, v41, v42\n"
  "v_mfma_f32_16x16x32_bf16 v[144:147], a[128:131], v[60:63], v[144:147]\n"
  "v_fma_f32 v37, v108, v40, v36\n"
  "v_fma_f32 v38, v109, v40, v36\n"
  "v_mfma_f32_16x16x32_bf16 v[52:55], a[8:11], v[60:63], v[52:55]\n"
  "v_cvt_pk_bf16_f32 v46, v37, v38\n"
  "v_fma_f32 v41, v110, v40, v36\n"
  "v_mfma_f32_16x16x32_bf16 v[152:155], a[32:35], v[60:63], v[152:155]\n"
  "s_waitcnt lgkmcnt(0)\n"
  "v_fma_f32 v42, v111, v40, v36\n"
  "v_cvt_pk_bf16_f32 v47, v41, v42\n"
  "v_mfma_f32_16x16x32_bf16 v[56:59], a[56:59], v[60:63], v[56:59]\n"
  "ds_read_b128 v[168:171], v252 offset:4096\n"
  "ds_write_b128 v251, v[44:47] offset:12288\n"
  "v_mfma_f32_16x16x32_bf16 v[140:143], a[84:87], v[148:151], v[140:143]\n"
  "v_fma_f32 v37, v32, v40, v36\n"
  "v_fma_f32 v38, v33, v40, v36\n"
  "v_mfma_f32_16x16x32_bf16 v[48:51], a[108:111], v[148:151], v[48:51]\n"
  "v_cvt_pk_bf16_f32 v232, v37, v38\n"
  "v_fma_f32 v41, v34, v40, v36\n"
  "v_mfma_f32_16x16x32_bf16 v[144:147], a[132:135], v[148:151], v[144:147]\n"
  "v_fma_f32 v42, v35, v40, v36\n"
  "v_cvt_pk_bf16_f32 v233, v41, v42\n"
  "v_mfma_f32_16x16x32_bf16 v[52:55], a[12:15], v[148:151], v[52:55]\n"
  "s_waitcnt lgkmcnt(1)\n"
  "ds_write_b64 v247, v[232:233] offset:12288\n"
  "v_mfma_f32_16x16x32_bf16 v[152:155], a[36:39], v[148:151], v[152:155]\n"
  "v_add_u32 v255, 32, v254\n"
  "v_bitop3_b32 v240, v236, v255, s91 bitop3:0x78\n"
  "v_mfma_f32_16x16x32_bf16 v[56:59], a[60:63], v[148:151], v[56:59]\n"
  "ds_read_b128 v[36:39], v252 offset:5120\n"
  "s_mov_b64 exec, s[54:55]\n"
  "buffer_load_dwordx4 v240, s[20:23], s50 offen nt lds\n"
  "s_mov_b64 exec, -1\n"
  "v_mfma_f32_16x16x32_bf16 v[140:143], a[88:91], v[168:171], v[140:143]\n"
  "s_add_u32 m0, s81, 51968\n"
  "v_add_u32 v255, 48, v254\n"
  "v_bitop3_b32 v240, v236, v255, s91 bitop3:0x78\n"
  "v_mfma_f32_16x16x32_bf16 v[48:51], a[112:115], v[168:171], v[48:51]\n"
  "s_mov_b64 exec, s[54:55]\n"
  "buffer_load_dwordx4 v240, s[20:23], s51 offen offset:3456 nt lds\n"
  "s_mov_b64 exec, -1\n"
  "v_mfma_f32_16x16x32_bf16 v[144:147], a[136:139], v[168:171], v[144:147]\n"
  "s_add_u32 m0, s81, 58880\n"
  "s_waitcnt lgkmcnt(1)\n"
  "s_barrier\n"
  "v_mfma_f32_16x16x32_bf16 v[52:55], a[16:19], v[168:171], v[52:55]\n"
  "s_waitcnt lgkmcnt(0)\n"
  "v_add_u32 v255, 64, v254\n"
  "v_bitop3_b32 v240, v236, v255, s91 bitop3:0x78\n"
  "v_mfma_f32_16x16x32_bf16 v[152:155], a[40:43], v[168:171], v[152:155]\n"
  "s_mov_b64 exec, s[54:55]\n"
  "buffer_load_dwordx4 v240, s[20:23], s52 offen nt lds\n"
  "s_mov_b64 exec, -1\n"
  "v_mfma_f32_16x16x32_bf16 v[56:59], a[64:67], v[168:171], v[56:59]\n"
  "v_mfma_f32_16x16x32_bf16 v[140:143], a[92:95], v[36:39], v[140:143]\n"
  "ds_read_b128 v[40:43], v252 offset:6144\n"
  "s_nop 0\n"
  "v_mfma_f32_16x16x32_bf16 v[48:51], a[116:119], v[36:39], v[48:51]\n"
  "s_nop 0\n"
  "s_nop 0\n"
  "v_mfma_f32_16x16x32_bf16 v[144:147], a[140:143], v[36:39], v[144:147]\n"
  "s_nop 0\n"
  "s_nop 0\n"
  "v_mfma_f32_16x16x32_bf16 v[52:55], a[20:23], v[36:39], v[52:55]\n"
  "ds_read_b128 v[44:47], v252 offset:7168\n"
  "v_cvt_pk_bf16_f32 v140, v140, v141\n"
  "v_mfma_f32_16x16x32_bf16 v[152:155], a[44:47], v[36:39], v[152:155]\n"
  "v_cvt_pk_bf16_f32 v141, v142, v143\n"
  "v_cvt_pk_bf16_f32 v142, v48, v49\n"
  "v_mfma_f32_16x16x32_bf16 v[56:59], a[68:71], v[36:39], v[56:59]\n"
  "s_waitcnt lgkmcnt(1)\n"
  "ds_read_b128 v[60:63], v252 offset:8192\n"
  "v_cvt_pk_bf16_f32 v143, v50, v51\n"
  "v_mfma_f32_16x16x32_bf16 v[36:39], a[72:75], v[40:43], 0\n"
  "v_cvt_pk_bf16_f32 v254, v144, v145\n"
  "v_cvt_pk_bf16_f32 v255, v146, v147\n"
  "v_mfma_f32_16x16x32_bf16 v[148:151], a[96:99], v[40:43], 0\n"
  "v_cvt_pk_bf16_f32 v52, v52, v53\n"
  "v_cvt_pk_bf16_f32 v53, v54, v55\n"
  "v_mfma_f32_16x16x32_bf16 v[144:147], a[120:123], v[40:43], 0\n"
  "s_waitcnt lgkmcnt(1)\n"
  "ds_read_b128 v[48:51], v252 offset:9216\n"
  "ds_read_u16 v240, v243 offset:0\n"
  "v_mfma_f32_16x16x32_bf16 v[36:39], a[76:79], v[44:47], v[36:39]\n"
  "ds_read_b128 v[40:43], v245 offset:0\n"
  "ds_read_b128 v[168:171], v245 offset:128\n"
  "v_mfma_f32_16x16x32_bf16 v[148:151], a[100:103], v[44:47], v[148:151]\n"
  "s_waitcnt lgkmcnt(4)\n"
  "ds_read_b128 v[172:175], v245 offset:256\n"
  "v_cvt_pk_bf16_f32 v54, v152, v153\n"
  "v_mfma_f32_16x16x32_bf16 v[144:147], a[124:127], v[44:47], v[144:147]\n"
  "ds_read_b128 v[176:179], v252 offset:10240\n"
  "v_cvt_pk_bf16_f32 v55, v154, v155\n"
  "v_mfma_f32_16x16x32_bf16 v[36:39], a[80:83], v[60:63], v[36:39]\n"
  "v_cvt_pk_bf16_f32 v236, v56, v57\n"
  "v_cvt_pk_bf16_f32 v237, v58, v59\n"
  "v_mfma_f32_16x16x32_bf16 v[148:151], a[104:107], v[60:63], v[148:151]\n"
  "s_waitcnt lgkmcnt(5)\n"
  "v_mfma_f32_16x16x32_bf16 v[144:147], a[128:131], v[60:63], v[144:147]\n"
  "s_waitcnt lgkmcnt(4)\n"
  "ds_read_b128 v[44:47], v252 offset:11264\n"
  "v_mad_u32_u24 v240, v240, s42, v242\n"
  "v_mfma_f32_16x16x32_bf16 v[36:39], a[84:87], v[48:51], v[36:39]\n"
  "s_waitcnt lgkmcnt(4)\n"
  "buffer_store_dwordx4 v[40:43], v240, s[24:27], s41 offen offset:0 sc1\n"
  "v_mfma_f32_16x16x32_bf16 v[148:151], a[108:111], v[48:51], v[148:151]\n"
  "s_waitcnt lgkmcnt(1)\n"
  "buffer_store_dwordx4 v[168:171], v240, s[24:27], s41 offen offset:128 sc1\n"
  "v_mfma_f32_16x16x32_bf16 v[144:147], a[132:135], v[48:51], v[144:147]\n"
  "s_waitcnt lgkmcnt(0)\n"
  "ds_read_b128 v[40:43], v252 offset:12288\n"
  "buffer_store_dwordx4 v[172:175], v240, s[24:27], s41 offen offset:256 sc1\n"
  "v_mfma_f32_16x16x32_bf16 v[36:39], a[88:91], v[176:179], v[36:39]\n"
  "v_mfma_f32_16x16x32_bf16 v[148:151], a[112:115], v[176:179], v[148:151]\n"
  "v_mfma_f32_16x16x32_bf16 v[144:147], a[136:139], v[176:179], v[144:147]\n"
  "s_waitcnt lgkmcnt(0)\n"
  "ds_read_b128 v[56:59], v252 offset:13312\n"
  "v_mfma_f32_16x16x32_bf16 v[36:39], a[92:95], v[44:47], v[36:39]\n"
  "s_nop 0\n"
  "s_nop 0\n"
  "v_mfma_f32_16x16x32_bf16 v[148:151], a[116:119], v[44:47], v[148:151]\n"
  "s_nop 0\n"
  "s_nop 0\n"
  "v_mfma_f32_16x16x32_bf16 v[144:147], a[140:143], v[44:47], v[144:147]\n"
  "ds_read_b128 v[60:63], v252 offset:14336\n"
  "s_nop 0\n"
  "v_mfma_f32_16x16x32_bf16 v[48:51], a[72:75], v[40:43], 0\n"
  "v_cvt_pk_bf16_f32 v36, v36, v37\n"
  "v_cvt_pk_bf16_f32 v37, v38, v39\n"
  "v_mfma_f32_16x16x32_bf16 v[44:47], a[96:99], v[40:43], 0\n"
  "s_waitcnt lgkmcnt(1)\n"
  "v_cvt_pk_bf16_f32 v38, v148, v149\n"
  "v_cvt_pk_bf16_f32 v39, v150, v151\n"
  "v_mfma_f32_16x16x32_bf16 v[152:155], a[120:123], v[40:43], 0\n"
  "ds_read_b128 v[168:171], v252 offset:15360\n"
  "v_cvt_pk_bf16_f32 v226, v144, v145\n"
  "v_mfma_f32_16x16x32_bf16 v[48:51], a[76:79], v[56:59], v[48:51]\n"
  "v_cvt_pk_bf16_f32 v227, v146, v147\n"
  "v_mfma_f32_16x16x32_bf16 v[44:47], a[100:103], v[56:59], v[44:47]\n"
  "s_waitcnt lgkmcnt(1)\n"
  "v_mfma_f32_16x16x32_bf16 v[152:155], a[124:127], v[56:59], v[152:155]\n"
  "ds_read_b128 v[40:43], v252 offset:16384\n"
  "v_mfma_f32_16x16x32_bf16 v[48:51], a[80:83], v[60:63], v[48:51]\n"
  "v_mfma_f32_16x16x32_bf16 v[44:47], a[104:107], v[60:63], v[44:47]\n"
  "s_waitcnt lgkmcnt(1)\n"
  "v_mfma_f32_16x16x32_bf16 v[152:155], a[128:131], v[60:63], v[152:155]\n"
  "ds_read_b128 v[56:59], v252 offset:17408\n"
  "v_mfma_f32_16x16x32_bf16 v[48:51], a[84:87], v[168:171], v[48:51]\n"
  "v_mfma_f32_16x16x32_bf16 v[44:47], a[108:111], v[168:171], v[44:47]\n"
  "s_waitcnt lgkmcnt(1)\n"
  "v_mfma_f32_16x16x32_bf16 v[152:155], a[132:135], v[168:171], v[152:155]\n"
  "v_mfma_f32_16x16x32_bf16 v[48:51], a[88:91], v[40:43], v[48:51]\n"
  "v_mfma_f32_16x16x32_bf16 v[44:47], a[112:115], v[40:43], v[44:47]\n"
  "s_waitcnt lgkmcnt(0)\n"
  "v_mfma_f32_16x16x32_bf16 v[152:155], a[136:139], v[40:43], v[152:155]\n"
  "v_mfma_f32_16x16x32_bf16 v[48:51], a[92:95], v[56:59], v[48:51]\n"
  "s_nop 0\n"
  "s_nop 0\n"
  "v_mfma_f32_16x16x32_bf16 v[44:47], a[116:119], v[56:59], v[44:47]\n"
  "s_nop 0\n"
  "s_nop 0\n"
  "v_mfma_f32_16x16x32_bf16 v[152:155], a[140:143], v[56:59], v[152:155]\n"
  "s_nop 0\n"
  "s_nop 0\n"
  "v_mfma_f32_16x16x32_bf16 v[40:43], v[228:231], v[112:115], v[10:13]\n"
  "v_cvt_pk_bf16_f32 v48, v48, v49\n"
  "v_cvt_pk_bf16_f32 v49, v50, v51\n"
  "v_mfma_f32_16x16x32_bf16 v[144:147], v[140:143], v[112:115], 0\n"
  "v_cvt_pk_bf16_f32 v50, v44, v45\n"
  "v_cvt_pk_bf16_f32 v51, v46, v47\n"
  "v_mfma_f32_16x16x32_bf16 v[148:151], v[36:39], v[112:115], 0\n"
  "v_cvt_pk_bf16_f32 v232, v152, v153\n"
  "v_cvt_pk_bf16_f32 v233, v154, v155\n"
  "v_mfma_f32_16x16x32_bf16 v[44:47], v[48:51], v[112:115], v[8:11]\n"
  "s_nop 0\n"
  "s_nop 0\n"
  "v_mfma_f32_16x16x16_bf16 v[40:43], v[238:239], v[234:235], v[40:43]\n"
  "s_nop 0\n"
  "s_nop 0\n"
  "v_mfma_f32_16x16x16_bf16 v[144:147], v[254:255], v[234:235], v[144:147]\n"
  "v_mfma_f32_16x16x16_bf16 v[148:151], v[226:227], v[234:235], v[148:151]\n"
  "v_mfma_f32_16x16x16_bf16 v[44:47], v[232:233], v[234:235], v[44:47]\n"
  "s_nop 0\n"
  "s_nop 0\n"
  "v_mfma_f32_16x16x32_bf16 v[60:63], v[228:231], v[52:55], v[10:13]\n"
  "s_nop 0\n"
  "s_nop 0\n"
  "v_mfma_f32_16x16x32_bf16 v[152:155], v[140:143], v[52:55], 0\n"
  "ds_read_b128 v[56:59], v252 offset:6144\n"
  "s_nop 0\n"
  "v_mfma_f32_16x16x32_bf16 v[168:171], v[36:39], v[52:55], 0\n"
  "v_max3_f32 v240, v44, v40, v41\n"
  "v_max3_f32 v240, v240, v42, v43\n"
  "v_mfma_f32_16x16x32_bf16 v[112:115], v[48:51], v[52:55], v[8:11]\n"
  "v_max3_f32 v240, v240, v144, v145\n"
  "v_max3_f32 v240, v240, v146, v147\n"
  "v_mfma_f32_16x16x16_bf16 v[60:63], v[238:239], v[236:237], v[60:63]\n"
  "ds_read_b128 v[52:55], v252 offset:7168\n"
  "v_max3_f32 v240, v240, v148, v149\n"
  "v_mfma_f32_16x16x16_bf16 v[152:155], v[254:255], v[236:237], v[152:155]\n"
  "v_max3_f32 v240, v240, v150, v151\n"
  "v_mfma_f32_16x16x16_bf16 v[168:171], v[226:227], v[236:237], v[168:171]\n"
  "s_waitcnt lgkmcnt(1)\n"
  "v_mfma_f32_16x16x16_bf16 v[112:115], v[232:233], v[236:237], v[112:115]\n"
  "ds_read_b128 v[172:175], v252 offset:8192\n"
  "s_nop 0\n"
  "v_mfma_f32_16x16x32_bf16 v[176:179], a[0:3], v[56:59], 0\n"
  "s_nop 0\n"
  "s_nop 0\n"
  "v_mfma_f32_16x16x32_bf16 v[180:183], a[24:27], v[56:59], 0\n"
  "s_waitcnt lgkmcnt(1)\n"
  "s_nop 0\n"
  "v_max3_f32 v237, v112, v60, v61\n"
  "v_mfma_f32_16x16x32_bf16 v[184:187], a[48:51], v[56:59], 0\n"
  "ds_read_b128 v[188:191], v252 offset:9216\n"
  "v_max3_f32 v237, v237, v62, v63\n"
  "v_mfma_f32_16x16x32_bf16 v[176:179], a[4:7], v[52:55], v[176:179]\n"
  "v_max3_f32 v237, v237, v152, v153\n"
  "v_max3_f32 v237, v237, v154, v155\n"
  "v_mfma_f32_16x16x32_bf16 v[180:183], a[28:31], v[52:55], v[180:183]\n"
  "s_waitcnt lgkmcnt(1)\n"
  "v_max3_f32 v237, v237, v168, v169\n"
  "v_max3_f32 v237, v237, v170, v171\n"
  "v_mfma_f32_16x16x32_bf16 v[184:187], a[52:55], v[52:55], v[184:187]\n"
  "ds_read_b128 v[56:59], v252 offset:10240\n"
  "v_mfma_f32_16x16x32_bf16 v[176:179], a[8:11], v[172:175], v[176:179]\n"
  "v_permlane16_swap_b32 v240, v237\n"
  "v_mfma_f32_16x16x32_bf16 v[180:183], a[32:35], v[172:175], v[180:183]\n"
  "s_waitcnt lgkmcnt(1)\n"
  "v_max_f32 v240, v240, v237\n"
  "v_mov_b32 v237, v240\n"
  "v_mfma_f32_16x16x32_bf16 v[184:187], a[56:59], v[172:175], v[184:187]\n"
  "ds_read_b128 v[52:55], v252 offset:11264\n"
  "v_mfma_f32_16x16x32_bf16 v[176:179], a[12:15], v[188:191], v[176:179]\n"
  "v_permlane32_swap_b32 v240, v237\n"
  "v_mfma_f32_16x16x32_bf16 v[180:183], a[36:39], v[188:191], v[180:183]\n"
  "s_waitcnt lgkmcnt(1)\n"
  "v_max_f32 v240, v240, v237\n"
  "v_mul_f32 v240, 0xbe553b95, v240\n"
  "v_mfma_f32_16x16x32_bf16 v[184:187], a[60:63], v[188:191], v[184:187]\n"
  "ds_read_b128 v[172:175], v252 offset:12288\n"
  "v_mov_b32 v237, v240\n"
  "v_mfma_f32_16x16x32_bf16 v[176:179], a[16:19], v[56:59], v[176:179]\n"
  "s_nop 0\n"
  "v_mfma_f32_16x16x32_bf16 v[180:183], a[40:43], v[56:59], v[180:183]\n"
  "s_waitcnt lgkmcnt(1)\n"
  "v_permlane16_swap_b32 v240, v237\n"
  "v_mfma_f32_16x16x32_bf16 v[184:187], a[64:67], v[56:59], v[184:187]\n"
  "ds_read_b128 v[188:191], v252 offset:13312\n"
  "v_fmamk_f32 v144, v144, 0x3e553b95, v240\n"
  "v_mfma_f32_16x16x32_bf16 v[176:179], a[20:23], v[52:55], v[176:179]\n"
  "v_exp_f32 v144, v144\n"
  "v_mfma_f32_16x16x32_bf16 v[180:183], a[44:47], v[52:55], v[180:183]\n"
  "s_waitcnt lgkmcnt(1)\n"
  "v_fmamk_f32 v145, v145, 0x3e553b95, v240\n"
  "v_fmamk_f32 v146, v146, 0x3e553b95, v240\n"
  "v_mfma_f32_16x16x32_bf16 v[184:187], a[68:71], v[52:55], v[184:187]\n"
  "ds_read_b128 v[56:59], v252 offset:14336\n"
  "v_fmamk_f32 v147, v147, 0x3e553b95, v240\n"
  "v_mfma_f32_16x16x32_bf16 v[52:55], a[0:3], v[172:175], 0\n"
  "v_cvt_pk_bf16_f32 v176, v176, v177\n"
  "v_cvt_pk_bf16_f32 v177, v178, v179\n"
  "v_mfma_f32_16x16x32_bf16 v[192:195], a[24:27], v[172:175], 0\n"
  "s_waitcnt lgkmcnt(1)\n"
  "v_cvt_pk_bf16_f32 v178, v180, v181\n"
  "v_cvt_pk_bf16_f32 v179, v182, v183\n"
  "v_mfma_f32_16x16x32_bf16 v[196:199], a[48:51], v[172:175], 0\n"
  "ds_read_b128 v[200:203], v252 offset:15360\n"
  "v_cvt_pk_bf16_f32 v234, v184, v185\n"
  "v_mfma_f32_16x16x32_bf16 v[52:55], a[4:7], v[188:191], v[52:55]\n"
  "v_cvt_pk_bf16_f32 v235, v186, v187\n"
  "v_fmamk_f32 v40, v40, 0x3e553b95, v240\n"
  "v_mfma_f32_16x16x32_bf16 v[192:195], a[28:31], v[188:191], v[192:195]\n"
  "s_waitcnt lgkmcnt(1)\n"
  "v_exp_f32 v145, v145\n"
  "v_mfma_f32_16x16x32_bf16 v[196:199], a[52:55], v[188:191], v[196:199]\n"
  "ds_read_b128 v[172:175], v252 offset:16384\n"
  "v_cvt_pk_bf16_f32 v144, v144, v145\n"
  "v_mfma_f32_16x16x32_bf16 v[52:55], a[8:11], v[56:59], v[52:55]\n"
  "v_exp_f32 v146, v146\n"
  "v_mfma_f32_16x16x32_bf16 v[192:195], a[32:35], v[56:59], v[192:195]\n"
  "s_waitcnt lgkmcnt(1)\n"
  "v_exp_f32 v147, v147\n"
  "v_mfma_f32_16x16x32_bf16 v[196:199], a[56:59], v[56:59], v[196:199]\n"
  "ds_read_b128 v[180:183], v252 offset:17408\n"
  "v_cvt_pk_bf16_f32 v145, v146, v147\n"
  "v_mfma_f32_16x16x32_bf16 v[52:55], a[12:15], v[200:203], v[52:55]\n"
  "v_exp_f32 v40, v40\n"
  "v_mfma_f32_16x16x32_bf16 v[192:195], a[36:39], v[200:203], v[192:195]\n"
  "s_waitcnt lgkmcnt(1)\n"
  "v_fmamk_f32 v41, v41, 0x3e553b95, v240\n"
  "v_fmamk_f32 v42, v42, 0x3e553b95, v240\n"
  "v_mfma_f32_16x16x32_bf16 v[196:199], a[60:63], v[200:203], v[196:199]\n"
  "v_exp_f32 v41, v41\n"
  "v_mfma_f32_16x16x32_bf16 v[52:55], a[16:19], v[172:175], v[52:55]\n"
  "v_exp_f32 v42, v42\n"
  "v_mfma_f32_16x16x32_bf16 v[192:195], a[40:43], v[172:175], v[192:195]\n"
  "s_waitcnt lgkmcnt(0)\n"
  "v_cvt_pk_bf16_f32 v146, v40, v41\n"
  "v_fmamk_f32 v43, v43, 0x3e553b95, v240\n"
  "v_mfma_f32_16x16x32_bf16 v[196:199], a[64:67], v[172:175], v[196:199]\n"
  "v_exp_f32 v43, v43\n"
  "v_mfma_f32_16x16x32_bf16 v[52:55], a[20:23], v[180:183], v[52:55]\n"
  "v_cvt_pk_bf16_f32 v147, v42, v43\n"
  "v_fmamk_f32 v236, v44, 0x3e553b95, v240\n"
  "v_mfma_f32_16x16x32_bf16 v[192:195], a[44:47], v[180:183], v[192:195]\n"
  "v_exp_f32 v236, v236\n"
  "v_mfma_f32_16x16x32_bf16 v[196:199], a[68:71], v[180:183], v[196:199]\n"
  "v_fmamk_f32 v148, v148, 0x3e553b95, v240\n"
  "v_fmamk_f32 v149, v149, 0x3e553b95, v240\n"
  "v_mfma_f32_16x16x32_bf16 v[172:175], v[228:231], v[176:179], v[10:13]\n"
  "v_cvt_pk_bf16_f32 v52, v52, v53\n"
  "v_cvt_pk_bf16_f32 v53, v54, v55\n"
  "v_mfma_f32_16x16x32_bf16 v[204:207], v[140:143], v[176:179], 0\n"
  "v_cvt_pk_bf16_f32 v54, v192, v193\n"
  "v_cvt_pk_bf16_f32 v55, v194, v195\n"
  "v_mfma_f32_16x16x32_bf16 v[40:43], v[36:39], v[176:179], 0\n"
  "v_cvt_pk_bf16_f32 v14, v196, v197\n"
  "v_cvt_pk_bf16_f32 v15, v198, v199\n"
  "v_mfma_f32_16x16x32_bf16 v[180:183], v[48:51], v[176:179], v[8:11]\n"
  "v_exp_f32 v148, v148\n"
  "v_mfma_f32_16x16x16_bf16 v[172:175], v[238:239], v[234:235], v[172:175]\n"
  "v_exp_f32 v149, v149\n"
  "v_mfma_f32_16x16x16_bf16 v[204:207], v[254:255], v[234:235], v[204:207]\n"
  "v_cvt_pk_bf16_f32 v148, v148, v149\n"
  "v_fmamk_f32 v150, v150, 0x3e553b95, v240\n"
  "v_mfma_f32_16x16x16_bf16 v[40:43], v[226:227], v[234:235], v[40:43]\n"
  "v_exp_f32 v150, v150\n"
  "v_mfma_f32_16x16x16_bf16 v[180:183], v[232:233], v[234:235], v[180:183]\n"
  "v_fmamk_f32 v151, v151, 0x3e553b95, v240\n"
  "v_fmamk_f32 v152, v152, 0x3e553b95, v237\n"
  "v_mfma_f32_16x16x32_bf16 v[176:179], v[228:231], v[52:55], v[10:13]\n"
  "v_exp_f32 v151, v151\n"
  "v_mfma_f32_16x16x32_bf16 v[44:47], v[140:143], v[52:55], 0\n"
  "ds_read_b128 v[184:187], v252 offset:0\n"
  "v_cvt_pk_bf16_f32 v149, v150, v151\n"
  "v_mfma_f32_16x16x32_bf16 v[56:59], v[36:39], v[52:55], 0\n"
  "v_exp_f32 v152, v152\n"
  "v_mfma_f32_16x16x32_bf16 v[188:191], v[48:51], v[52:55], v[8:11]\n"
  "v_cvt_pk_bf16_f32 v150, v236, 0\n"
  "v_mov_b32 v151, 0\n"
  "v_mfma_f32_16x16x16_bf16 v[176:179], v[238:239], v[14:15], v[176:179]\n"
  "ds_read_b128 v[36:39], v252 offset:1024\n"
  "v_fmamk_f32 v153, v153, 0x3e553b95, v237\n"
  "v_mfma_f32_16x16x16_bf16 v[44:47], v[254:255], v[14:15], v[44:47]\n"
  "v_exp_f32 v153, v153\n"
  "v_mfma_f32_16x16x16_bf16 v[56:59], v[226:227], v[14:15], v[56:59]\n"
  "s_waitcnt lgkmcnt(1)\n"
  "v_cvt_pk_bf16_f32 v152, v152, v153\n"
  "v_fmamk_f32 v154, v154, 0x3e553b95, v237\n"
  "v_mfma_f32_16x16x16_bf16 v[188:191], v[232:233], v[14:15], v[188:191]\n"
  "ds_read_b128 v[140:143], v252 offset:2048\n"
  "v_fmamk_f32 v155, v155, 0x3e553b95, v237\n"
  "v_mfma_f32_16x16x32_bf16 v[192:195], v[184:187], a[144:147], 0\n"
  "v_exp_f32 v154, v154\n"
  "v_mfma_f32_16x16x32_bf16 v[196:199], v[184:187], a[168:171], 0\n"
  "s_waitcnt lgkmcnt(1)\n"
  "v_exp_f32 v155, v155\n"
  "v_mfma_f32_16x16x32_bf16 v[200:203], v[184:187], a[192:195], 0\n"
  "ds_read_b128 v[208:211], v252 offset:3072\n"
  "v_cvt_pk_bf16_f32 v153, v154, v155\n"
  "v_mfma_f32_16x16x32_bf16 v[192:195], v[36:39], a[148:151], v[192:195]\n"
  "v_fmamk_f32 v60, v60, 0x3e553b95, v237\n"
  "v_fmamk_f32 v61, v61, 0x3e553b95, v237\n"
  "v_mfma_f32_16x16x32_bf16 v[196:199], v[36:39], a[172:175], v[196:199]\n"
  "s_waitcnt lgkmcnt(1)\n"
  "v_exp_f32 v60, v60\n"
  "v_mfma_f32_16x16x32_bf16 v[200:203], v[36:39], a[196:199], v[200:203]\n"
  "ds_read_b128 v[184:187], v252 offset:4096\n"
  "v_fmamk_f32 v62, v62, 0x3e553b95, v237\n"
  "v_mfma_f32_16x16x32_bf16 v[192:195], v[140:143], a[152:155], v[192:195]\n"
  "v_exp_f32 v61, v61\n"
  "v_mfma_f32_16x16x32_bf16 v[196:199], v[140:143], a[176:179], v[196:199]\n"
  "s_waitcnt lgkmcnt(1)\n"
  "v_exp_f32 v62, v62\n"
  "v_mfma_f32_16x16x32_bf16 v[200:203], v[140:143], a[200:203], v[200:203]\n"
  "ds_read_b128 v[36:39], v252 offset:5120\n"
  "v_cvt_pk_bf16_f32 v154, v60, v61\n"
  "v_mfma_f32_16x16x32_bf16 v[192:195], v[208:211], a[156:159], v[192:195]\n"
  "v_fmamk_f32 v63, v63, 0x3e553b95, v237\n"
  "v_fmamk_f32 v255, v112, 0x3e553b95, v237\n"
  "v_mfma_f32_16x16x32_bf16 v[196:199], v[208:211], a[180:183], v[196:199]\n"
  "s_waitcnt lgkmcnt(1)\n"
  "v_exp_f32 v63, v63\n"
  "v_mfma_f32_16x16x32_bf16 v[200:203], v[208:211], a[204:207], v[200:203]\n"
  "ds_read_b128 v[112:115], v252 offset:6144\n"
  "v_cvt_pk_bf16_f32 v155, v62, v63\n"
  "v_mfma_f32_16x16x32_bf16 v[192:195], v[184:187], a[160:163], v[192:195]\n"
  "v_exp_f32 v255, v255\n"
  "v_mfma_f32_16x16x32_bf16 v[196:199], v[184:187], a[184:187], v[196:199]\n"
  "s_waitcnt lgkmcnt(1)\n"
  "v_fmamk_f32 v168, v168, 0x3e553b95, v237\n"
  "v_fmamk_f32 v169, v169, 0x3e553b95, v237\n"
  "v_mfma_f32_16x16x32_bf16 v[200:203], v[184:187], a[208:211], v[200:203]\n"
  "ds_read_b128 v[60:63], v252 offset:7168\n"
  "v_fmamk_f32 v170, v170, 0x3e553b95, v237\n"
  "v_mfma_f32_16x16x32_bf16 v[192:195], v[36:39], a[164:167], v[192:195]\n"
  "v_exp_f32 v168, v168\n"
  "v_mfma_f32_16x16x32_bf16 v[196:199], v[36:39], a[188:191], v[196:199]\n"
  "s_waitcnt lgkmcnt(1)\n"
  "v_exp_f32 v169, v169\n"
  "v_mfma_f32_16x16x32_bf16 v[200:203], v[36:39], a[212:215], v[200:203]\n"
  "ds_read_b128 v[140:143], v252 offset:8192\n"
  "v_cvt_pk_bf16_f32 v168, v168, v169\n"
  "v_mfma_f32_16x16x32_bf16 v[36:39], v[112:115], a[144:147], 0\n"
  "v_cvt_pk_bf16_f32 v156, v192, v193\n"
  "v_cvt_pk_bf16_f32 v157, v194, v195\n"
  "v_mfma_f32_16x16x32_bf16 v[184:187], v[112:115], a[168:171], 0\n"
  "s_waitcnt lgkmcnt(1)\n"
  "v_cvt_pk_bf16_f32 v160, v196, v197\n"
  "v_cvt_pk_bf16_f32 v161, v198, v199\n"
  "v_mfma_f32_16x16x32_bf16 v[192:195], v[112:115], a[192:195], 0\n"
  "ds_read_b128 v[208:211], v252 offset:9216\n"
  "v_cvt_pk_bf16_f32 v164, v200, v201\n"
  "v_mfma_f32_16x16x32_bf16 v[36:39], v[60:63], a[148:151], v[36:39]\n"
  "v_cvt_pk_bf16_f32 v165, v202, v203\n"
  "v_fmamk_f32 v171, v171, 0x3e553b95, v237\n"
  "v_mfma_f32_16x16x32_bf16 v[184:187], v[60:63], a[172:175], v[184:187]\n"
  "s_waitcnt lgkmcnt(1)\n"
  "v_exp_f32 v170, v170\n"
  "v_mfma_f32_16x16x32_bf16 v[192:195], v[60:63], a[196:199], v[192:195]\n"
  "ds_read_b128 v[112:115], v252 offset:10240\n"
  "v_max3_f32 v238, v180, v172, v173\n"
  "v_mfma_f32_16x16x32_bf16 v[36:39], v[140:143], a[152:155], v[36:39]\n"
  "v_exp_f32 v171, v171\n"
  "v_mfma_f32_16x16x32_bf16 v[184:187], v[140:143], a[176:179], v[184:187]\n"
  "s_waitcnt lgkmcnt(1)\n"
  "v_cvt_pk_bf16_f32 v169, v170, v171\n"
  "v_cvt_pk_bf16_f32 v170, v255, 0\n"
  "v_mfma_f32_16x16x32_bf16 v[192:195], v[140:143], a[200:203], v[192:195]\n"
  "ds_read_b128 v[60:63], v252 offset:11264\n"
  "v_mov_b32 v171, 0\n"
  "v_mfma_f32_16x16x32_bf16 v[36:39], v[208:211], a[156:159], v[36:39]\n"
  "v_max3_f32 v238, v238, v174, v175\n"
  "v_max3_f32 v238, v238, v204, v205\n"
  "v_mfma_f32_16x16x32_bf16 v[184:187], v[208:211], a[180:183], v[184:187]\n"
  "s_waitcnt lgkmcnt(1)\n"
  "v_max3_f32 v238, v238, v206, v207\n"
  "v_max3_f32 v238, v238, v40, v41\n"
  "v_mfma_f32_16x16x32_bf16 v[192:195], v[208:211], a[204:207], v[192:195]\n"
  "ds_read_b128 v[196:199], v252 offset:12288\n"
  "v_max3_f32 v238, v238, v42, v43\n"
  "v_mfma_f32_16x16x32_bf16 v[36:39], v[112:115], a[160:163], v[36:39]\n"
  "v_max3_f32 v235, v188, v176, v177\n"
  "v_max3_f32 v235, v235, v178, v179\n"
  "v_mfma_f32_16x16x32_bf16 v[184:187], v[112:115], a[184:187], v[184:187]\n"
  "s_waitcnt lgkmcnt(1)\n"
  "v_max3_f32 v235, v235, v44, v45\n"
  "v_max3_f32 v235, v235, v46, v47\n"
  "v_mfma_f32_16x16x32_bf16 v[192:195], v[112:115], a[208:211], v[192:195]\n"
  "ds_read_b128 v[200:203], v252 offset:13312\n"
  "v_max3_f32 v235, v235, v56, v57\n"
  "v_mfma_f32_16x16x32_bf16 v[36:39], v[60:63], a[164:167], v[36:39]\n"
  "v_max3_f32 v235, v235, v58, v59\n"
  "v_xor_b32 v10, 0xff800000, v8\n"
  "v_mfma_f32_16x16x32_bf16 v[184:187], v[60:63], a[188:191], v[184:187]\n"
  "s_waitcnt lgkmcnt(1)\n"
  "v_permlane16_swap_b32 v238, v235\n"
  "v_mfma_f32_16x16x32_bf16 v[192:195], v[60:63], a[212:215], v[192:195]\n"
  "ds_read_b128 v[208:211], v252 offset:14336\n"
  "v_max_f32 v238, v238, v235\n"
  "v_mfma_f32_16x16x32_bf16 v[212:215], v[196:199], a[144:147], 0\n"
  "v_cvt_pk_bf16_f32 v60, v36, v37\n"
  "v_cvt_pk_bf16_f32 v61, v38, v39\n"
  "v_mfma_f32_16x16x32_bf16 v[216:219], v[196:199], a[168:171], 0\n"
  "s_waitcnt lgkmcnt(1)\n"
  "v_cvt_pk_bf16_f32 v112, v184, v185\n"
  "v_cvt_pk_bf16_f32 v113, v186, v187\n"
  "v_mfma_f32_16x16x32_bf16 v[36:39], v[196:199], a[192:195], 0\n"
  "ds_read_b128 v[220:223], v252 offset:15360\n"
  "v_cvt_pk_bf16_f32 v140, v192, v193\n"
  "v_mfma_f32_16x16x32_bf16 v[212:215], v[200:203], a[148:151], v[212:215]\n"
  "v_cvt_pk_bf16_f32 v141, v194, v195\n"
  "v_mov_b32 v235, v238\n"
  "v_mfma_f32_16x16x32_bf16 v[216:219], v[200:203], a[172:175], v[216:219]\n"
  "s_waitcnt lgkmcnt(1)\n"
  "v_permlane32_swap_b32 v238, v235\n"
  "v_mfma_f32_16x16x32_bf16 v[36:39], v[200:203], a[196:199], v[36:39]\n"
  "ds_read_b128 v[184:187], v252 offset:16384\n"
  "v_max_f32 v238, v238, v235\n"
  "v_mfma_f32_16x16x32_bf16 v[212:215], v[208:211], a[152:155], v[212:215]\n"
  "v_mul_f32 v238, 0xbe553b95, v238\n"
  "v_mov_b32 v235, v238\n"
  "v_mfma_f32_16x16x32_bf16 v[216:219], v[208:211], a[176:179], v[216:219]\n"
  "s_waitcnt lgkmcnt(1)\n"
  "v_permlane16_swap_b32 v238, v235\n"
  "v_mfma_f32_16x16x32_bf16 v[36:39], v[208:211], a[200:203], v[36:39]\n"
  "ds_read_b128 v[192:195], v252 offset:17408\n"
  "v_fmamk_f32 v204, v204, 0x3e553b95, v238\n"
  "v_mfma_f32_16x16x32_bf16 v[212:215], v[220:223], a[156:159], v[212:215]\n"
  "v_exp_f32 v204, v204\n"
  "v_mfma_f32_16x16x32_bf16 v[216:219], v[220:223], a[180:183], v[216:219]\n"
  "s_waitcnt lgkmcnt(1)\n"
  "v_fmamk_f32 v205, v205, 0x3e553b95, v238\n"
  "v_fmamk_f32 v206, v206, 0x3e553b95, v238\n"
  "v_mfma_f32_16x16x32_bf16 v[36:39], v[220:223], a[204:207], v[36:39]\n"
  "v_exp_f32 v205, v205\n"
  "v_mfma_f32_16x16x32_bf16 v[212:215], v[184:187], a[160:163], v[212:215]\n"
  "v_exp_f32 v206, v206\n"
  "v_mfma_f32_16x16x32_bf16 v[216:219], v[184:187], a[184:187], v[216:219]\n"
  "s_waitcnt lgkmcnt(0)\n"
  "v_cvt_pk_bf16_f32 v204, v204, v205\n"
  "v_fmamk_f32 v207, v207, 0x3e553b95, v238\n"
  "v_mfma_f32_16x16x32_bf16 v[36:39], v[184:187], a[208:211], v[36:39]\n"
  "v_exp_f32 v207, v207\n"
  "v_mfma_f32_16x16x32_bf16 v[212:215], v[192:195], a[164:167], v[212:215]\n"
  "v_cvt_pk_bf16_f32 v205, v206, v207\n"
  "v_fmamk_f32 v172, v172, 0x3e553b95, v238\n"
  "v_mfma_f32_16x16x32_bf16 v[216:219], v[192:195], a[188:191], v[216:219]\n"
  "v_exp_f32 v172, v172\n"
  "v_mfma_f32_16x16x32_bf16 v[36:39], v[192:195], a[212:215], v[36:39]\n"
  "ds_read_u16 v240, v253 offset:2062\n"
  "v_fmamk_f32 v173, v173, 0x3e553b95, v238\n"
  "v_mfma_f32_16x16x32_bf16 v[184:187], v[16:19], v[144:147], 0\n"
  "v_cvt_pk_bf16_f32 v62, v212, v213\n"
  "v_cvt_pk_bf16_f32 v63, v214, v215\n"
  "v_mfma_f32_16x16x32_bf16 v[192:195], v[156:159], v[144:147], 0\n"
  "v_cvt_pk_bf16_f32 v114, v216, v217\n"
  "v_cvt_pk_bf16_f32 v115, v218, v219\n"
  "v_mfma_f32_16x16x32_bf16 v[196:199], v[160:163], v[144:147], 0\n"
  "v_cvt_pk_bf16_f32 v142, v36, v37\n"
  "v_cvt_pk_bf16_f32 v143, v38, v39\n"
  "v_mfma_f32_16x16x32_bf16 v[200:203], v[164:167], v[144:147], 0\n"
  "v_exp_f32 v173, v173\n"
  "v_mfma_f32_16x16x32_bf16 v[184:187], v[16:19], v[148:151], v[184:187]\n"
  "s_waitcnt lgkmcnt(0)\n"
  "v_add_u32 v234, v240, v250\n"
  "v_add_u32 v255, v240, v249\n"
  "v_mfma_f32_16x16x32_bf16 v[192:195], v[60:63], v[148:151], v[192:195]\n"
  "ds_read_u16 v237, v253 offset:2304\n"
  "v_add_u32 v254, v240, v248\n"
  "v_mfma_f32_16x16x32_bf16 v[196:199], v[112:115], v[148:151], v[196:199]\n"
  "v_cvt_pk_bf16_f32 v206, v172, v173\n"
  "v_fmamk_f32 v174, v174, 0x3e553b95, v238\n"
  "v_mfma_f32_16x16x32_bf16 v[200:203], v[140:143], v[148:151], v[200:203]\n"
  "v_rcp_f32 v184, v184\n"
  "v_mfma_f32_16x16x32_bf16 v[36:39], v[16:19], v[152:155], 0\n"
  "v_mul_f32 v192, v192, v184\n"
  "v_mul_f32 v193, v193, v184\n"
  "v_mfma_f32_16x16x32_bf16 v[148:151], v[156:159], v[152:155], 0\n"
  "v_cvt_pk_bf16_f32 v192, v192, v193\n"
  "v_mul_f32 v194, v194, v184\n"
  "v_mfma_f32_16x16x32_bf16 v[208:211], v[160:163], v[152:155], 0\n"
  "s_waitcnt lgkmcnt(0)\n"
  "v_mul_f32 v195, v195, v184\n"
  "v_cvt_pk_bf16_f32 v193, v194, v195\n"
  "v_mfma_f32_16x16x32_bf16 v[212:215], v[164:167], v[152:155], 0\n"
  "v_mul_f32 v196, v196, v184\n"
  "v_mul_f32 v197, v197, v184\n"
  "v_mfma_f32_16x16x32_bf16 v[36:39], v[16:19], v[168:171], v[36:39]\n"
  "ds_read_b128 v[152:155], v234 offset:0\n"
  "v_cvt_pk_bf16_f32 v194, v196, v197\n"
  "v_mfma_f32_16x16x32_bf16 v[148:151], v[60:63], v[168:171], v[148:151]\n"
  "v_mul_f32 v198, v198, v184\n"
  "v_mul_f32 v199, v199, v184\n"
  "v_mfma_f32_16x16x32_bf16 v[208:211], v[112:115], v[168:171], v[208:211]\n"
  "ds_read_b128 v[216:219], v255 offset:0\n"
  "ds_read_b128 v[220:223], v255 offset:3072\n"
  "v_mfma_f32_16x16x32_bf16 v[212:215], v[140:143], v[168:171], v[212:215]\n"
  "v_cvt_pk_bf16_f32 v195, v198, v199\n"
  "v_mul_f32 v200, v200, v184\n"
  "v_mul_f32 v201, v201, v184\n"
  "v_cvt_pk_bf16_f32 v200, v200, v201\n"
  "v_rcp_f32 v36, v36\n"
  "v_mul_f32 v202, v202, v184\n"
  "v_mul_f32 v203, v203, v184\n"
  "s_waitcnt lgkmcnt(2)\n"
  "v_mfma_f32_16x16x32_bf16 v[144:147], v[24:27], v[152:155], 0\n"
  "v_cvt_pk_bf16_f32 v201, v202, v203\n"
  "s_mov_b64 exec, s[56:57]\n"
  "ds_write_b128 v251, v[192:195] offset:46080\n"
  "ds_write_b64 v247, v[200:201] offset:46080\n"
  "s_mov_b64 exec, -1\n"
  "s_waitcnt lgkmcnt(2)\n"
  "v_smfmac_f32_16x16x64_bf16 v[144:147], v[20:23], v[216:223], v9\n"
  "v_mul_f32 v148, v148, v36\n"
  "v_mul_f32 v149, v149, v36\n"
  "v_cvt_pk_bf16_f32 v148, v148, v149\n"
  "v_mul_f32 v150, v150, v36\n"
  "v_mul_f32 v151, v151, v36\n"
  "v_cvt_pk_bf16_f32 v149, v150, v151\n"
  "v_mul_f32 v208, v208, v36\n"
  "v_mul_f32 v209, v209, v36\n"
  "v_cvt_pk_bf16_f32 v150, v208, v209\n"
  "v_mul_f32 v210, v210, v36\n"
  "v_mul_f32 v211, v211, v36\n"
  "v_cvt_pk_bf16_f32 v151, v210, v211\n"
  "ds_write_b128 v251, v[148:151] offset:27648\n"
  "v_mul_f32 v212, v212, v36\n"
  "v_mul_f32 v213, v213, v36\n"
  "v_cvt_pk_bf16_f32 v212, v212, v213\n"
  "v_mul_f32 v214, v214, v36\n"
  "v_mul_f32 v215, v215, v36\n"
  "v_cvt_pk_bf16_f32 v213, v214, v215\n"
  "ds_write_b64 v247, v[212:213] offset:27648\n"
  "v_add_u32 v3, v237, v250\n"
  "v_add_u32 v240, v237, v249\n"
  "v_add_u32 v239, v237, v248\n"
  "v_exp_f32 v174, v174\n"
  "v_fmamk_f32 v175, v175, 0x3e553b95, v238\n"
  "v_exp_f32 v175, v175\n"
  "v_fmamk_f32 v237, v180, 0x3e553b95, v238\n"
  "v_cvt_pk_bf16_f32 v207, v174, v175\n"
  "v_exp_f32 v237, v237\n"
  "v_fmamk_f32 v40, v40, 0x3e553b95, v238\n"
  "v_exp_f32 v40, v40\n"
  "v_fmamk_f32 v41, v41, 0x3e553b95, v238\n"
  "v_exp_f32 v41, v41\n"
  "v_fmamk_f32 v42, v42, 0x3e553b95, v238\n"
  "v_cvt_pk_bf16_f32 v40, v40, v41\n"
  "v_exp_f32 v42, v42\n"
  "s_waitcnt vmcnt(3) lgkmcnt(0)\n"
  "s_barrier\n"
  "ds_read_b128 v[36:39], v252 offset:46080\n"
  "ds_read_b128 v[148:151], v254 offset:0\n"
  "ds_read_b128 v[152:155], v254 offset:3072\n"
  "v_fmamk_f32 v43, v43, 0x3e553b95, v238\n"
  "v_exp_f32 v43, v43\n"
  "v_fmamk_f32 v44, v44, 0x3e553b95, v235\n"
  "v_cvt_pk_bf16_f32 v41, v42, v43\n"
  "v_cvt_pk_bf16_f32 v42, v237, 0\n"
  "v_mov_b32 v43, 0\n"
  "v_exp_f32 v44, v44\n"
  "v_fmamk_f32 v45, v45, 0x3e553b95, v235\n"
  "v_exp_f32 v45, v45\n"
  "v_fmamk_f32 v46, v46, 0x3e553b95, v235\n"
  "v_exp_f32 v46, v46\n"
  "v_cvt_pk_bf16_f32 v44, v44, v45\n"
  "v_fmamk_f32 v47, v47, 0x3e553b95, v235\n"
  "s_waitcnt lgkmcnt(2)\n"
  "v_mfma_f32_16x16x32_bf16 v[64:67], a[216:219], v[36:39], v[64:67]\n"
  "ds_read_b128 v[168:171], v252 offset:47104\n"
  "ds_read_b128 v[172:175], v3 offset:0\n"
  "v_mfma_f32_16x16x32_bf16 v[68:71], a[232:235], v[36:39], v[68:71]\n"
  "s_waitcnt lgkmcnt(3)\n"
  "ds_read_u16 v231, v253 offset:2306\n"
  "v_fmamk_f32 v176, v176, 0x3e553b95, v235\n"
  "v_mfma_f32_16x16x32_bf16 v[72:75], a[244:247], v[36:39], v[72:75]\n"
  "s_waitcnt lgkmcnt(3)\n"
  "ds_read_b128 v[180:183], v240 offset:0\n"
  "ds_read_b128 v[184:187], v240 offset:3072\n"
  "v_smfmac_f32_16x16x64_bf16 v[144:147], v[28:31], v[148:155], v9\n"
  "ds_read_b128 v[192:195], v252 offset:48128\n"
  "v_exp_f32 v47, v47\n"
  "v_exp_f32 v176, v176\n"
  "v_cvt_pk_bf16_f32 v45, v46, v47\n"
  "v_fmamk_f32 v177, v177, 0x3e553b95, v235\n"
  "v_fmamk_f32 v178, v178, 0x3e553b95, v235\n"
  "s_waitcnt lgkmcnt(5)\n"
  "v_mfma_f32_16x16x32_bf16 v[64:67], a[220:223], v[168:171], v[64:67]\n"
  "ds_read_b128 v[196:199], v239 offset:0\n"
  "ds_read_b128 v[200:203], v239 offset:3072\n"
  "s_waitcnt lgkmcnt(6)\n"
  "v_mfma_f32_16x16x32_bf16 v[148:151], v[24:27], v[172:175], 0\n"
  "s_waitcnt lgkmcnt(5)\n"
  "v_add_u32 v238, v231, v250\n"
  "v_add_u32 v237, v231, v249\n"
  "v_mfma_f32_16x16x32_bf16 v[68:71], a[236:239], v[168:171], v[68:71]\n"
  "s_waitcnt lgkmcnt(4)\n"
  "ds_read_b128 v[172:175], v238 offset:0\n"
  "v_add_u32 v236, v231, v248\n"
  "v_mfma_f32_16x16x32_bf16 v[72:75], a[248:251], v[168:171], v[72:75]\n"
  "s_waitcnt lgkmcnt(4)\n"
  "v_exp_f32 v177, v177\n"
  "v_smfmac_f32_16x16x64_bf16 v[148:151], v[20:23], v[180:187], v9\n"
  "s_waitcnt lgkmcnt(3)\n"
  "ds_read_b128 v[208:211], v237 offset:0\n"
  "ds_read_b128 v[212:215], v237 offset:3072\n"
  "v_mfma_f32_16x16x32_bf16 v[64:67], a[224:227], v[192:195], v[64:67]\n"
  "s_waitcnt lgkmcnt(4)\n"
  "ds_read_b128 v[180:183], v252 offset:49152\n"
  "v_cvt_pk_bf16_f32 v46, v176, v177\n"
  "v_mfma_f32_16x16x32_bf16 v[68:71], a[240:243], v[192:195], v[68:71]\n"
  "s_waitcnt lgkmcnt(4)\n"
  "v_exp_f32 v178, v178\n"
  "v_smfmac_f32_16x16x64_bf16 v[148:151], v[28:31], v[196:203], v9\n"
  "s_waitcnt lgkmcnt(3)\n"
  "ds_read_b128 v[216:219], v236 offset:0\n"
  "ds_read_b128 v[220:223], v236 offset:3072\n"
  "v_mfma_f32_16x16x32_bf16 v[36:39], v[24:27], v[172:175], 0\n"
  "ds_read_b128 v[152:155], v241 offset:8192\n"
  "v_fmamk_f32 v179, v179, 0x3e553b95, v235\n"
  "v_mfma_f32_16x16x32_bf16 v[72:75], a[252:255], v[192:195], v[72:75]\n"
  "s_waitcnt lgkmcnt(5)\n"
  "ds_read_b128 v[184:187], v234 offset:128\n"
  "v_fmamk_f32 v2, v188, 0x3e553b95, v235\n"
  "v_mfma_f32_16x16x32_bf16 v[168:171], v[16:19], v[204:207], 0\n"
  "s_waitcnt lgkmcnt(5)\n"
  "v_exp_f32 v179, v179\n"
  "v_smfmac_f32_16x16x64_bf16 v[36:39], v[20:23], v[208:215], v9\n"
  "s_waitcnt lgkmcnt(4)\n"
  "ds_read_b128 v[172:175], v241 offset:0\n"
  "v_cvt_pk_bf16_f32 v47, v178, v179\n"
  "v_mfma_f32_16x16x32_bf16 v[64:67], a[228:231], v[180:183], v[64:67]\n"
  "s_waitcnt lgkmcnt(4)\n"
  "ds_read_b128 v[208:211], v255 offset:128\n"
  "ds_read_b128 v[212:215], v255 offset:3200\n"
  "v_mfma_f32_16x16x32_bf16 v[68:71], v[4:7], v[180:183], v[68:71]\n"
  "s_waitcnt lgkmcnt(5)\n"
  "ds_read_b128 v[176:179], v241 offset:4096\n"
  "v_fmamk_f32 v56, v56, 0x3e553b95, v235\n"
  "v_smfmac_f32_16x16x64_bf16 v[36:39], v[28:31], v[216:223], v9\n"
  "s_waitcnt lgkmcnt(5)\n"
  "ds_read_b128 v[224:227], v254 offset:128\n"
  "v_fmamk_f32 v57, v57, 0x3e553b95, v235\n"
  "v_mfma_f32_16x16x32_bf16 v[72:75], v[76:79], v[180:183], v[72:75]\n"
  "s_waitcnt lgkmcnt(5)\n"
  "ds_read_b128 v[228:231], v254 offset:3200\n"
  "v_fmamk_f32 v58, v58, 0x3e553b95, v235\n"
  "v_mfma_f32_16x16x32_bf16 v[180:183], v[152:155], v[184:187], 0\n"
  "s_waitcnt lgkmcnt(5)\n"
  "ds_read_b128 v[220:223], v3 offset:128\n"
  "v_fmamk_f32 v59, v59, 0x3e553b95, v235\n"
  "v_mfma_f32_16x16x32_bf16 v[184:187], v[156:159], v[204:207], 0\n"
  "s_waitcnt lgkmcnt(5)\n"
  "v_exp_f32 v2, v2\n"
  "v_mfma_f32_16x16x32_bf16 v[188:191], v[160:163], v[204:207], 0\n"
  "s_waitcnt lgkmcnt(4)\n"
  "v_exp_f32 v56, v56\n"
  "v_smfmac_f32_16x16x64_bf16 v[180:183], v[172:175], v[208:215], v9\n"
  "s_waitcnt lgkmcnt(3)\n"
  "ds_read_b128 v[192:195], v240 offset:128\n"
  "ds_read_b128 v[196:199], v240 offset:3200\n"
  "v_mfma_f32_16x16x32_bf16 v[200:203], v[164:167], v[204:207], 0\n"
  "s_waitcnt lgkmcnt(4)\n"
  "v_exp_f32 v57, v57\n"
  "v_mfma_f32_16x16x32_bf16 v[168:171], v[16:19], v[40:43], v[168:171]\n"
  "s_waitcnt lgkmcnt(3)\n"
  "v_exp_f32 v58, v58\n"
  "v_smfmac_f32_16x16x64_bf16 v[180:183], v[176:179], v[224:231], v9\n"
  "s_waitcnt lgkmcnt(2)\n"
  "ds_read_b128 v[204:207], v239 offset:128\n"
  "ds_read_b128 v[208:211], v239 offset:3200\n"
  "v_mfma_f32_16x16x32_bf16 v[212:215], v[152:155], v[220:223], 0\n"
  "ds_read_b128 v[216:219], v238 offset:128\n"
  "v_cvt_pk_bf16_f32 v56, v56, v57\n"
  "v_mfma_f32_16x16x32_bf16 v[184:187], v[60:63], v[40:43], v[184:187]\n"
  "s_waitcnt lgkmcnt(4)\n"
  "v_exp_f32 v59, v59\n"
  "v_mfma_f32_16x16x32_bf16 v[188:191], v[112:115], v[40:43], v[188:191]\n"
  "s_waitcnt lgkmcnt(3)\n"
  "v_cvt_pk_bf16_f32 v57, v58, v59\n"
  "v_cvt_pk_bf16_f32 v58, v2, 0\n"
  "v_smfmac_f32_16x16x64_bf16 v[212:215], v[172:175], v[192:199], v9\n"
  "ds_read_b128 v[220:223], v237 offset:128\n"
  "ds_read_b128 v[224:227], v237 offset:3200\n"
  "v_mfma_f32_16x16x32_bf16 v[200:203], v[140:143], v[40:43], v[200:203]\n"
  "s_waitcnt lgkmcnt(4)\n"
  "v_mov_b32 v59, 0\n"
  "v_add_f32 v235, v144, v145\n"
  "v_mfma_f32_16x16x32_bf16 v[192:195], v[16:19], v[44:47], 0\n"
  "s_waitcnt lgkmcnt(3)\n"
  "v_mul_f32 v2, v144, v144\n"
  "v_fmac_f32 v2, v145, v145\n"
  "v_smfmac_f32_16x16x64_bf16 v[212:215], v[176:179], v[204:211], v9\n"
  "s_waitcnt lgkmcnt(2)\n"
  "ds_read_b128 v[204:207], v236 offset:128\n"
  "ds_read_b128 v[208:211], v236 offset:3200\n"
  "v_mfma_f32_16x16x32_bf16 v[40:43], v[152:155], v[216:219], 0\n"
  "ds_read_b128 v[196:199], v234 offset:256\n"
  "v_add_f32 v235, v146, v235\n"
  "v_mfma_f32_16x16x32_bf16 v[152:155], v[156:159], v[44:47], 0\n"
  "s_waitcnt lgkmcnt(4)\n"
  "ds_read_b128 v[216:219], v241 offset:20480\n"
  "v_fmac_f32 v2, v146, v146\n"
  "v_mfma_f32_16x16x32_bf16 v[156:159], v[160:163], v[44:47], 0\n"
  "s_waitcnt lgkmcnt(4)\n"
  "v_add_f32 v235, v147, v235\n"
  "v_fmac_f32 v2, v147, v147\n"
  "v_smfmac_f32_16x16x64_bf16 v[40:43], v[172:175], v[220:227], v9\n"
  "ds_read_b128 v[220:223], v255 offset:256\n"
  "ds_read_b128 v[224:227], v255 offset:3328\n"
  "v_mfma_f32_16x16x32_bf16 v[160:163], v[164:167], v[44:47], 0\n"
  "s_waitcnt lgkmcnt(5)\n"
  "ds_read_b128 v[172:175], v241 offset:12288\n"
  "v_add_f32 v235, v180, v235\n"
  "v_mfma_f32_16x16x32_bf16 v[192:195], v[16:19], v[56:59], v[192:195]\n"
  "s_waitcnt lgkmcnt(5)\n"
  "v_fmac_f32 v2, v180, v180\n"
  "v_add_f32 v235, v181, v235\n"
  "v_smfmac_f32_16x16x64_bf16 v[40:43], v[176:179], v[204:211], v9\n"
  "s_waitcnt lgkmcnt(4)\n"
  "ds_read_b128 v[204:207], v254 offset:256\n"
  "ds_read_b128 v[208:211], v254 offset:3328\n"
  "v_mfma_f32_16x16x32_bf16 v[152:155], v[60:63], v[56:59], v[152:155]\n"
  "s_waitcnt lgkmcnt(5)\n"
  "ds_read_b128 v[164:167], v241 offset:16384\n"
  "v_fmac_f32 v2, v181, v181\n"
  "v_mfma_f32_16x16x32_bf16 v[176:179], v[216:219], v[196:199], 0\n"
  "s_waitcnt lgkmcnt(5)\n"
  "ds_read_b128 v[228:231], v3 offset:256\n"
  "v_add_f32 v235, v182, v235\n"
  "v_mfma_f32_16x16x32_bf16 v[156:159], v[112:115], v[56:59], v[156:159]\n"
  "s_waitcnt lgkmcnt(5)\n"
  "v_fmac_f32 v2, v182, v182\n"
  "v_add_f32 v235, v183, v235\n"
  "v_mfma_f32_16x16x32_bf16 v[160:163], v[140:143], v[56:59], v[160:163]\n"
  "s_waitcnt lgkmcnt(4)\n"
  "v_fmac_f32 v2, v183, v183\n"
  "v_add_f32 v255, v148, v149\n"
  "v_smfmac_f32_16x16x64_bf16 v[176:179], v[172:175], v[220:227], v9\n"
  "s_waitcnt lgkmcnt(3)\n"
  "ds_read_b128 v[220:223], v240 offset:256\n"
  "ds_read_b128 v[224:227], v240 offset:3328\n"
  "ds_read_b128 v[44:47], v252 offset:50176\n"
  "v_mul_f32 v254, v148, v148\n"
  "v_fmac_f32 v254, v149, v149\n"
  "v_add_f32 v255, v150, v255\n"
  "v_fmac_f32 v254, v150, v150\n"
  "v_add_f32 v255, v151, v255\n"
  "s_waitcnt lgkmcnt(4)\n"
  "v_smfmac_f32_16x16x64_bf16 v[176:179], v[164:167], v[204:211], v9\n"
  "ds_read_b128 v[204:207], v239 offset:256\n"
  "ds_read_b128 v[208:211], v239 offset:3328\n"
  "s_waitcnt lgkmcnt(5)\n"
  "v_mfma_f32_16x16x32_bf16 v[56:59], v[216:219], v[228:231], 0\n"
  "ds_read_b128 v[196:199], v238 offset:256\n"
  "v_fmac_f32 v254, v151, v151\n"
  "v_add_f32 v255, v212, v255\n"
  "v_fmac_f32 v254, v212, v212\n"
  "v_add_f32 v235, v176, v235\n"
  "v_fmac_f32 v2, v176, v176\n"
  "v_add_f32 v235, v177, v235\n"
  "v_fmac_f32 v2, v177, v177\n"
  "s_waitcnt lgkmcnt(4)\n"
  "v_smfmac_f32_16x16x64_bf16 v[56:59], v[172:175], v[220:227], v9\n"
  "ds_read_b128 v[228:231], v252 offset:51200\n"
  "v_add_f32 v235, v178, v235\n"
  "s_waitcnt lgkmcnt(4)\n"
  "v_mfma_f32_16x16x32_bf16 v[64:67], v[80:83], v[44:47], v[64:67]\n"
  "s_waitcnt lgkmcnt(3)\n"
  "ds_read_b128 v[220:223], v237 offset:256\n"
  "ds_read_b128 v[224:227], v237 offset:3328\n"
  "v_mfma_f32_16x16x32_bf16 v[68:71], v[84:87], v[44:47], v[68:71]\n"
  "s_waitcnt lgkmcnt(4)\n"
  "v_fmac_f32 v2, v178, v178\n"
  "v_add_f32 v235, v179, v235\n"
  "v_smfmac_f32_16x16x64_bf16 v[56:59], v[164:167], v[204:211], v9\n"
  "s_waitcnt lgkmcnt(3)\n"
  "ds_read_b128 v[204:207], v236 offset:256\n"
  "ds_read_b128 v[208:211], v236 offset:3328\n"
  "v_mfma_f32_16x16x32_bf16 v[72:75], v[88:91], v[44:47], v[72:75]\n"
  "ds_read_b128 v[236:239], v252 offset:27648\n"
  "v_fmac_f32 v2, v179, v179\n"
  "v_mfma_f32_16x16x32_bf16 v[44:47], v[216:219], v[196:199], 0\n"
  "s_waitcnt lgkmcnt(5)\n"
  "v_permlane16_swap_b32 v235, v2\n"
  "v_mfma_f32_16x16x32_bf16 v[64:67], v[92:95], v[228:231], v[64:67]\n"
  "s_waitcnt lgkmcnt(4)\n"
  "v_add_f32 v235, v235, v2\n"
  "v_add_f32 v255, v213, v255\n"
  "v_mfma_f32_16x16x32_bf16 v[68:71], v[96:99], v[228:231], v[68:71]\n"
  "s_waitcnt lgkmcnt(3)\n"
  "v_fmac_f32 v254, v213, v213\n"
  "v_add_f32 v255, v214, v255\n"
  "v_smfmac_f32_16x16x64_bf16 v[44:47], v[172:175], v[220:227], v9\n"
  "s_waitcnt lgkmcnt(2)\n"
  "ds_read_b128 v[196:199], v252 offset:28672\n"
  "v_cvt_pk_bf16_f32 v64, v64, v65\n"
  "v_mfma_f32_16x16x32_bf16 v[72:75], v[100:103], v[228:231], v[72:75]\n"
  "s_waitcnt lgkmcnt(2)\n"
  "v_cvt_pk_bf16_f32 v65, v66, v67\n"
  "v_cvt_pk_bf16_f32 v68, v68, v69\n"
  "v_smfmac_f32_16x16x64_bf16 v[44:47], v[164:167], v[204:211], v9\n"
  "s_waitcnt lgkmcnt(1)\n"
  "ds_write_b64 v244, v[64:65] offset:18432\n"
  "v_mfma_f32_16x16x32_bf16 v[116:119], a[216:219], v[236:239], v[116:119]\n"
  "v_cvt_pk_bf16_f32 v69, v70, v71\n"
  "v_cvt_pk_bf16_f32 v72, v72, v73\n"
  "v_mfma_f32_16x16x32_bf16 v[120:123], a[232:235], v[236:239], v[120:123]\n"
  "ds_write_b64 v244, v[68:69] offset:18560\n"
  "v_mfma_f32_16x16x32_bf16 v[124:127], a[244:247], v[236:239], v[124:127]\n"
  "s_waitcnt lgkmcnt(2)\n"
  "v_cvt_pk_bf16_f32 v73, v74, v75\n"
  "v_fmac_f32 v254, v214, v214\n"
  "v_mfma_f32_16x16x32_bf16 v[116:119], a[220:223], v[196:199], v[116:119]\n"
  "ds_write_b64 v244, v[72:73] offset:18688\n"
  "v_mfma_f32_16x16x32_bf16 v[120:123], a[236:239], v[196:199], v[120:123]\n"
  "v_add_f32 v255, v215, v255\n"
  "v_fmac_f32 v254, v215, v215\n"
  "v_mfma_f32_16x16x32_bf16 v[124:127], a[248:251], v[196:199], v[124:127]\n"
  "ds_read_b128 v[64:67], v252 offset:29696\n"
  "ds_read_b128 v[68:71], v252 offset:30720\n"
  "v_add_f32 v255, v56, v255\n"
  "v_fmac_f32 v254, v56, v56\n"
  "v_add_f32 v255, v57, v255\n"
  "v_fmac_f32 v254, v57, v57\n"
  "v_add_f32 v255, v58, v255\n"
  "v_fmac_f32 v254, v58, v58\n"
  "v_add_f32 v255, v59, v255\n"
  "v_fmac_f32 v254, v59, v59\n"
  "v_add_f32 v240, v36, v37\n"
  "v_mul_f32 v239, v36, v36\n"
  "v_permlane16_swap_b32 v255, v254\n"
  "v_add_f32 v255, v255, v254\n"
  "v_fmac_f32 v239, v37, v37\n"
  "v_add_f32 v240, v38, v240\n"
  "v_permlane32_swap_b32 v235, v255\n"
  "v_add_f32 v235, v235, v255\n"
  "s_waitcnt lgkmcnt(1)\n"
  "v_mfma_f32_16x16x32_bf16 v[116:119], a[224:227], v[64:67], v[116:119]\n"
  "ds_write_b32 v246, v235 offset:0\n"
  "v_mfma_f32_16x16x32_bf16 v[120:123], a[240:243], v[64:67], v[120:123]\n"
  "s_waitcnt lgkmcnt(1)\n"
  "v_fmac_f32 v239, v38, v38\n"
  "v_add_f32 v240, v39, v240\n"
  "v_mfma_f32_16x16x32_bf16 v[124:127], a[252:255], v[64:67], v[124:127]\n"
  "ds_read_b128 v[72:75], v252 offset:31744\n"
  "v_fmac_f32 v239, v39, v39\n"
  "v_mfma_f32_16x16x32_bf16 v[116:119], a[228:231], v[68:71], v[116:119]\n"
  "v_add_f32 v240, v40, v240\n"
  "v_fmac_f32 v239, v40, v40\n"
  "v_mfma_f32_16x16x32_bf16 v[120:123], v[4:7], v[68:71], v[120:123]\n"
  "v_add_f32 v240, v41, v240\n"
  "v_fmac_f32 v239, v41, v41\n"
  "v_mfma_f32_16x16x32_bf16 v[124:127], v[76:79], v[68:71], v[124:127]\n"
  "v_add_f32 v240, v42, v240\n"
  "v_fmac_f32 v239, v42, v42\n"
  "v_add_f32 v240, v43, v240\n"
  "v_fmac_f32 v239, v43, v43\n"
  "v_add_f32 v240, v44, v240\n"
  "v_fmac_f32 v239, v44, v44\n"
  "v_add_f32 v240, v45, v240\n"
  "v_fmac_f32 v239, v45, v45\n"
  "s_waitcnt lgkmcnt(0)\n"
  "v_mfma_f32_16x16x32_bf16 v[116:119], v[80:83], v[72:75], v[116:119]\n"
  "v_add_f32 v240, v46, v240\n"
  "v_fmac_f32 v239, v46, v46\n"
  "v_mfma_f32_16x16x32_bf16 v[120:123], v[84:87], v[72:75], v[120:123]\n"
  "v_add_f32 v240, v47, v240\n"
  "v_fmac_f32 v239, v47, v47\n"
  "v_mfma_f32_16x16x32_bf16 v[124:127], v[88:91], v[72:75], v[124:127]\n"
  "v_rcp_f32 v168, v168\n"
  "v_permlane16_swap_b32 v240, v239\n"
  "v_add_f32 v240, v240, v239\n"
  "v_mov_b32 v255, v240\n"
  "v_mul_f32 v184, v184, v168\n"
  "v_mul_f32 v185, v185, v168\n"
  "v_permlane32_swap_b32 v240, v255\n"
  "v_add_f32 v240, v240, v255\n"
  "ds_write_b32 v246, v240 offset:1024\n"
  "v_cvt_pk_bf16_f32 v184, v184, v185\n"
  "v_mul_f32 v186, v186, v168\n"
  "v_mul_f32 v187, v187, v168\n"
  "v_cvt_pk_bf16_f32 v185, v186, v187\n"
  "v_mul_f32 v188, v188, v168\n"
  "v_mul_f32 v189, v189, v168\n"
  "v_cvt_pk_bf16_f32 v186, v188, v189\n"
  "v_mul_f32 v190, v190, v168\n"
  "v_mul_f32 v191, v191, v168\n"
  "v_cvt_pk_bf16_f32 v187, v190, v191\n"
  "v_mul_f32 v200, v200, v168\n"
  "v_mul_f32 v201, v201, v168\n"
  "v_cvt_pk_bf16_f32 v200, v200, v201\n"
  "v_mul_f32 v202, v202, v168\n"
  "v_mul_f32 v203, v203, v168\n"
  "v_cvt_pk_bf16_f32 v201, v202, v203\n"
  "v_rcp_f32 v192, v192\n"
  "s_mov_b64 exec, s[72:73]\n"
  "v_mov_b32 v8, 0\n"
  "s_mov_b64 exec, -1\n"
  "s_waitcnt lgkmcnt(0)\n"
  "s_barrier\n"
  "ds_read_b128 v[64:67], v252 offset:32768\n"
  "ds_write_b128 v251, v[184:187] offset:33792\n"
  "v_mul_f32 v152, v152, v192\n"
  "v_mul_f32 v153, v153, v192\n"
  "ds_write_b64 v247, v[200:201] offset:33792\n"
  "v_cvt_pk_bf16_f32 v152, v152, v153\n"
  "v_mul_f32 v154, v154, v192\n"
  "ds_read_b128 v[68:71], v253 offset:0\n"
  "s_waitcnt lgkmcnt(3)\n"
  "v_mfma_f32_16x16x32_bf16 v[116:119], v[92:95], v[64:67], v[116:119]\n"
  "ds_read_b128 v[72:75], v253 offset:256\n"
  "v_mul_f32 v155, v155, v192\n"
  "v_mfma_f32_16x16x32_bf16 v[120:123], v[96:99], v[64:67], v[120:123]\n"
  "ds_read_b128 v[164:167], v253 offset:512\n"
  "v_cvt_pk_bf16_f32 v153, v154, v155\n"
  "v_mfma_f32_16x16x32_bf16 v[124:127], v[100:103], v[64:67], v[124:127]\n"
  "ds_read_b128 v[168:171], v253 offset:768\n"
  "v_mul_f32 v156, v156, v192\n"
  "v_cvt_pk_bf16_f32 v116, v116, v117\n"
  "v_cvt_pk_bf16_f32 v117, v118, v119\n"
  "ds_write_b64 v244, v[116:117] offset:0\n"
  "v_cvt_pk_bf16_f32 v120, v120, v121\n"
  "v_cvt_pk_bf16_f32 v121, v122, v123\n"
  "ds_write_b64 v244, v[120:121] offset:128\n"
  "s_waitcnt lgkmcnt(5)\n"
  "v_add_f32 v68, v68, v69\n"
  "v_add_f32 v70, v70, v71\n"
  "v_add_f32 v68, v68, v70\n"
  "s_waitcnt lgkmcnt(4)\n"
  "v_add_f32 v72, v72, v73\n"
  "v_add_f32 v74, v74, v75\n"
  "v_add_f32 v72, v72, v74\n"
  "v_mul_f32 v68, 0xbbaaaaab, v68\n"
  "v_mul_f32 v72, 0x3baaaaab, v72\n"
  "v_fma_f32 v72, -v68, v68, v72\n"
  "v_add_f32_e64 v72, abs(v72), s43\n"
  "v_rsq_f32 v72, v72\n"
  "v_cvt_pk_bf16_f32 v124, v124, v125\n"
  "v_mul_f32 v68, v68, v72\n"
  "v_fma_f32 v69, v144, v72, v68\n"
  "v_fma_f32 v70, v145, v72, v68\n"
  "v_cvt_pk_bf16_f32 v64, v69, v70\n"
  "v_fma_f32 v73, v146, v72, v68\n"
  "v_fma_f32 v74, v147, v72, v68\n"
  "v_cvt_pk_bf16_f32 v65, v73, v74\n"
  "v_fma_f32 v69, v180, v72, v68\n"
  "v_fma_f32 v70, v181, v72, v68\n"
  "v_cvt_pk_bf16_f32 v66, v69, v70\n"
  "v_fma_f32 v73, v182, v72, v68\n"
  "v_fma_f32 v74, v183, v72, v68\n"
  "v_cvt_pk_bf16_f32 v67, v73, v74\n"
  "ds_write_b128 v251, v[64:67] offset:18432\n"
  "v_fma_f32 v69, v176, v72, v68\n"
  "v_fma_f32 v70, v177, v72, v68\n"
  "v_cvt_pk_bf16_f32 v254, v69, v70\n"
  "v_fma_f32 v73, v178, v72, v68\n"
  "v_fma_f32 v74, v179, v72, v68\n"
  "v_cvt_pk_bf16_f32 v255, v73, v74\n"
  "ds_write_b64 v247, v[254:255] offset:18432\n"
  "v_cvt_pk_bf16_f32 v125, v126, v127\n"
  "ds_write_b64 v244, v[124:125] offset:256\n"
  "v_mul_f32 v157, v157, v192\n"
  "v_cvt_pk_bf16_f32 v154, v156, v157\n"
  "v_mul_f32 v158, v158, v192\n"
  "v_mul_f32 v159, v159, v192\n"
  "v_cvt_pk_bf16_f32 v155, v158, v159\n"
  "v_mul_f32 v160, v160, v192\n"
  "v_mul_f32 v161, v161, v192\n"
  "v_cvt_pk_bf16_f32 v160, v160, v161\n"
  "v_mul_f32 v162, v162, v192\n"
  "v_mul_f32 v163, v163, v192\n"
  "v_cvt_pk_bf16_f32 v161, v162, v163\n"
  "s_mov_b64 exec, s[64:65]\n"
  "ds_write_b128 v251, v[152:155] offset:39936\n"
  "ds_write_b64 v247, v[160:161] offset:39936\n"
  "s_mov_b64 exec, -1\n"
  "s_waitcnt lgkmcnt(3)\n"
  "s_barrier\n"
  "ds_read_b128 v[64:67], v252 offset:33792\n"
  "ds_read_b128 v[68:71], v252 offset:34816\n"
  "ds_read_b128 v[72:75], v252 offset:35840\n"
  "v_add_f32 v164, v164, v165\n"
  "v_add_f32 v166, v166, v167\n"
  "v_add_f32 v164, v164, v166\n"
  "v_add_f32 v168, v168, v169\n"
  "v_add_f32 v170, v170, v171\n"
  "v_add_f32 v168, v168, v170\n"
  "v_mul_f32 v164, 0xbbaaaaab, v164\n"
  "v_mul_f32 v168, 0x3baaaaab, v168\n"
  "v_fma_f32 v168, -v164, v164, v168\n"
  "v_add_f32_e64 v168, abs(v168), s43\n"
  "v_rsq_f32 v168, v168\n"
  "s_nop 0\n"
  "v_mul_f32 v164, v164, v168\n"
  "v_fma_f32 v165, v148, v168, v164\n"
  "v_fma_f32 v166, v149, v168, v164\n"
  "v_cvt_pk_bf16_f32 v116, v165, v166\n"
  "s_waitcnt lgkmcnt(2)\n"
  "v_mfma_f32_16x16x32_bf16 v[128:131], a[216:219], v[64:67], v[128:131]\n"
  "ds_read_b128 v[120:123], v253 offset:1024\n"
  "ds_read_b128 v[124:127], v253 offset:1280\n"
  "v_mfma_f32_16x16x32_bf16 v[132:135], a[232:235], v[64:67], v[132:135]\n"
  "s_waitcnt lgkmcnt(3)\n"
  "v_fma_f32 v169, v150, v168, v164\n"
  "v_fma_f32 v170, v151, v168, v164\n"
  "v_mfma_f32_16x16x32_bf16 v[136:139], a[244:247], v[64:67], v[136:139]\n"
  "s_waitcnt lgkmcnt(2)\n"
  "ds_read_b128 v[152:155], v252 offset:36864\n"
  "v_cvt_pk_bf16_f32 v117, v169, v170\n"
  "v_mfma_f32_16x16x32_bf16 v[128:131], a[220:223], v[68:71], v[128:131]\n"
  "v_fma_f32 v165, v212, v168, v164\n"
  "v_fma_f32 v166, v213, v168, v164\n"
  "v_mfma_f32_16x16x32_bf16 v[132:135], a[236:239], v[68:71], v[132:135]\n"
  "v_cvt_pk_bf16_f32 v118, v165, v166\n"
  "v_fma_f32 v169, v214, v168, v164\n"
  "v_mfma_f32_16x16x32_bf16 v[136:139], a[248:251], v[68:71], v[136:139]\n"
  "s_waitcnt lgkmcnt(2)\n"
  "ds_read_b128 v[64:67], v252 offset:37888\n"
  "v_fma_f32 v170, v215, v168, v164\n"
  "v_mfma_f32_16x16x32_bf16 v[128:131], a[224:227], v[72:75], v[128:131]\n"
  "s_waitcnt lgkmcnt(2)\n"
  "v_cvt_pk_bf16_f32 v119, v169, v170\n"
  "v_fma_f32 v165, v56, v168, v164\n"
  "v_mfma_f32_16x16x32_bf16 v[132:135], a[240:243], v[72:75], v[132:135]\n"
  "s_waitcnt lgkmcnt(1)\n"
  "ds_write_b128 v251, v[116:119] offset:0\n"
  "v_mfma_f32_16x16x32_bf16 v[136:139], a[252:255], v[72:75], v[136:139]\n"
  "v_fma_f32 v166, v57, v168, v164\n"
  "v_cvt_pk_bf16_f32 v254, v165, v166\n"
  "v_mfma_f32_16x16x32_bf16 v[128:131], a[228:231], v[152:155], v[128:131]\n"
  "ds_read_b128 v[68:71], v252 offset:38912\n"
  "v_fma_f32 v169, v58, v168, v164\n"
  "v_mfma_f32_16x16x32_bf16 v[132:135], v[4:7], v[152:155], v[132:135]\n"
  "s_waitcnt lgkmcnt(2)\n"
  "v_fma_f32 v170, v59, v168, v164\n"
  "v_cvt_pk_bf16_f32 v255, v169, v170\n"
  "v_mfma_f32_16x16x32_bf16 v[136:139], v[76:79], v[152:155], v[136:139]\n"
  "ds_read_b128 v[72:75], v252 offset:18432\n"
  "ds_write_b64 v247, v[254:255] offset:0\n"
  "v_mfma_f32_16x16x32_bf16 v[128:131], v[80:83], v[64:67], v[128:131]\n"
  "v_add_f32 v120, v120, v121\n"
  "v_add_f32 v122, v122, v123\n"
  "v_mfma_f32_16x16x32_bf16 v[132:135], v[84:87], v[64:67], v[132:135]\n"
  "v_add_f32 v120, v120, v122\n"
  "v_add_f32 v124, v124, v125\n"
  "v_mfma_f32_16x16x32_bf16 v[136:139], v[88:91], v[64:67], v[136:139]\n"
  "s_waitcnt lgkmcnt(2)\n"
  "v_add_f32 v126, v126, v127\n"
  "v_add_f32 v124, v124, v126\n"
  "v_mfma_f32_16x16x32_bf16 v[128:131], v[92:95], v[68:71], v[128:131]\n"
  "v_mul_f32 v120, 0xbbaaaaab, v120\n"
  "v_mul_f32 v124, 0x3baaaaab, v124\n"
  "v_mfma_f32_16x16x32_bf16 v[132:135], v[96:99], v[68:71], v[132:135]\n"
  "s_waitcnt lgkmcnt(1)\n"
  "v_fma_f32 v124, -v120, v120, v124\n"
  "v_add_f32_e64 v124, abs(v124), s43\n"
  "v_mfma_f32_16x16x32_bf16 v[136:139], v[100:103], v[68:71], v[136:139]\n"
  "ds_read_b128 v[64:67], v252 offset:19456\n"
  "v_cvt_pk_bf16_f32 v128, v128, v129\n"
  "v_mfma_f32_16x16x32_bf16 v[68:71], a[72:75], v[72:75], 0\n"
  "v_cvt_pk_bf16_f32 v129, v130, v131\n"
  "v_cvt_pk_bf16_f32 v132, v132, v133\n"
  "v_mfma_f32_16x16x32_bf16 v[116:119], a[96:99], v[72:75], 0\n"
  "ds_write_b64 v244, v[128:129] offset:6144\n"
  "v_mfma_f32_16x16x32_bf16 v[152:155], a[120:123], v[72:75], 0\n"
  "v_cvt_pk_bf16_f32 v133, v134, v135\n"
  "v_cvt_pk_bf16_f32 v136, v136, v137\n"
  "v_mfma_f32_16x16x32_bf16 v[128:131], a[0:3], v[72:75], 0\n"
  "ds_write_b64 v244, v[132:133] offset:6272\n"
  "v_mfma_f32_16x16x32_bf16 v[156:159], a[24:27], v[72:75], 0\n"
  "s_waitcnt lgkmcnt(2)\n"
  "v_cvt_pk_bf16_f32 v137, v138, v139\n"
  "v_mfma_f32_16x16x32_bf16 v[132:135], a[48:51], v[72:75], 0\n"
  "ds_write_b64 v244, v[136:137] offset:6400\n"
  "v_mfma_f32_16x16x32_bf16 v[68:71], a[76:79], v[64:67], v[68:71]\n"
  "v_rsq_f32 v124, v124\n"
  "v_mfma_f32_16x16x32_bf16 v[116:119], a[100:103], v[64:67], v[116:119]\n"
  "ds_read_b128 v[72:75], v252 offset:20480\n"
  "v_mul_f32 v120, v120, v124\n"
  "v_mfma_f32_16x16x32_bf16 v[152:155], a[124:127], v[64:67], v[152:155]\n"
  "v_fma_f32 v121, v36, v124, v120\n"
  "v_fma_f32 v122, v37, v124, v120\n"
  "v_mfma_f32_16x16x32_bf16 v[128:131], a[4:7], v[64:67], v[128:131]\n"
  "v_cvt_pk_bf16_f32 v136, v121, v122\n"
  "v_fma_f32 v125, v38, v124, v120\n"
  "v_mfma_f32_16x16x32_bf16 v[156:159], a[28:31], v[64:67], v[156:159]\n"
  "v_fma_f32 v126, v39, v124, v120\n"
  "v_cvt_pk_bf16_f32 v137, v125, v126\n"
  "v_mfma_f32_16x16x32_bf16 v[132:135], a[52:55], v[64:67], v[132:135]\n"
  "ds_read_b128 v[160:163], v252 offset:21504\n"
  "v_fma_f32 v121, v40, v124, v120\n"
  "v_fma_f32 v122, v41, v124, v120\n"
  "v_cvt_pk_bf16_f32 v138, v121, v122\n"
  "s_waitcnt lgkmcnt(1)\n"
  "v_mfma_f32_16x16x32_bf16 v[68:71], a[80:83], v[72:75], v[68:71]\n"
  "v_fma_f32 v125, v42, v124, v120\n"
  "v_fma_f32 v126, v43, v124, v120\n"
  "v_mfma_f32_16x16x32_bf16 v[116:119], a[104:107], v[72:75], v[116:119]\n"
  "v_cvt_pk_bf16_f32 v139, v125, v126\n"
  "v_fma_f32 v121, v44, v124, v120\n"
  "v_mfma_f32_16x16x32_bf16 v[152:155], a[128:131], v[72:75], v[152:155]\n"
  "ds_write_b128 v251, v[136:139] offset:6144\n"
  "v_mfma_f32_16x16x32_bf16 v[128:131], a[8:11], v[72:75], v[128:131]\n"
  "s_waitcnt lgkmcnt(1)\n"
  "v_fma_f32 v122, v45, v124, v120\n"
  "v_cvt_pk_bf16_f32 v254, v121, v122\n"
  "v_mfma_f32_16x16x32_bf16 v[156:159], a[32:35], v[72:75], v[156:159]\n"
  "v_fma_f32 v125, v46, v124, v120\n"
  "v_fma_f32 v126, v47, v124, v120\n"
  "v_mfma_f32_16x16x32_bf16 v[132:135], a[56:59], v[72:75], v[132:135]\n"
  "ds_read_b128 v[64:67], v252 offset:22528\n"
  "v_cvt_pk_bf16_f32 v255, v125, v126\n"
  "v_mfma_f32_16x16x32_bf16 v[68:71], a[84:87], v[160:163], v[68:71]\n"
  "ds_write_b64 v247, v[254:255] offset:6144\n"
  "v_mfma_f32_16x16x32_bf16 v[116:119], a[108:111], v[160:163], v[116:119]\n"
  "v_mfma_f32_16x16x32_bf16 v[152:155], a[132:135], v[160:163], v[152:155]\n"
  "v_mfma_f32_16x16x32_bf16 v[128:131], a[12:15], v[160:163], v[128:131]\n"
  "v_mfma_f32_16x16x32_bf16 v[156:159], a[36:39], v[160:163], v[156:159]\n"
  "s_waitcnt lgkmcnt(1)\n"
  "v_mfma_f32_16x16x32_bf16 v[132:135], a[60:63], v[160:163], v[132:135]\n"
  "ds_read_b128 v[72:75], v252 offset:23552\n"
  "v_mfma_f32_16x16x32_bf16 v[68:71], a[88:91], v[64:67], v[68:71]\n"
  "s_waitcnt lgkmcnt(1)\n"
  "s_barrier\n"
  "v_mfma_f32_16x16x32_bf16 v[116:119], a[112:115], v[64:67], v[116:119]\n"
  "v_mfma_f32_16x16x32_bf16 v[152:155], a[136:139], v[64:67], v[152:155]\n"
  "v_mfma_f32_16x16x32_bf16 v[128:131], a[16:19], v[64:67], v[128:131]\n"
  "v_mfma_f32_16x16x32_bf16 v[156:159], a[40:43], v[64:67], v[156:159]\n"
  "s_waitcnt lgkmcnt(0)\n"
  "v_mfma_f32_16x16x32_bf16 v[132:135], a[64:67], v[64:67], v[132:135]\n"
  "v_mfma_f32_16x16x32_bf16 v[68:71], a[92:95], v[72:75], v[68:71]\n"
  "ds_read_b128 v[120:123], v252 offset:0\n"
  "s_nop 0\n"
  "v_mfma_f32_16x16x32_bf16 v[116:119], a[116:119], v[72:75], v[116:119]\n"
  "s_nop 0\n"
  "s_nop 0\n"
  "v_mfma_f32_16x16x32_bf16 v[152:155], a[140:143], v[72:75], v[152:155]\n"
  "s_nop 0\n"
  "s_nop 0\n"
  "v_mfma_f32_16x16x32_bf16 v[128:131], a[20:23], v[72:75], v[128:131]\n"
  "ds_read_b128 v[64:67], v252 offset:1024\n"
  "v_cvt_pk_bf16_f32 v68, v68, v69\n"
  "v_mfma_f32_16x16x32_bf16 v[156:159], a[44:47], v[72:75], v[156:159]\n"
  "v_cvt_pk_bf16_f32 v69, v70, v71\n"
  "v_cvt_pk_bf16_f32 v70, v116, v117\n"
  "v_mfma_f32_16x16x32_bf16 v[132:135], a[68:71], v[72:75], v[132:135]\n"
  "s_waitcnt lgkmcnt(1)\n"
  "ds_read_b128 v[124:127], v252 offset:2048\n"
  "v_cvt_pk_bf16_f32 v71, v118, v119\n"
  "v_mfma_f32_16x16x32_bf16 v[72:75], a[72:75], v[120:123], 0\n"
  "v_cvt_pk_bf16_f32 v254, v152, v153\n"
  "v_cvt_pk_bf16_f32 v255, v154, v155\n"
  "v_mfma_f32_16x16x32_bf16 v[116:119], a[96:99], v[120:123], 0\n"
  "v_cvt_pk_bf16_f32 v128, v128, v129\n"
  "v_cvt_pk_bf16_f32 v129, v130, v131\n"
  "v_mfma_f32_16x16x32_bf16 v[136:139], a[120:123], v[120:123], 0\n"
  "s_waitcnt lgkmcnt(1)\n"
  "ds_read_b128 v[152:155], v252 offset:3072\n"
  "ds_read_u16 v240, v243 offset:8\n"
  "v_mfma_f32_16x16x32_bf16 v[72:75], a[76:79], v[64:67], v[72:75]\n"
  "ds_read_b128 v[120:123], v245 offset:12288\n"
  "ds_read_b128 v[160:163], v245 offset:12416\n"
  "v_mfma_f32_16x16x32_bf16 v[116:119], a[100:103], v[64:67], v[116:119]\n"
  "s_waitcnt lgkmcnt(4)\n"
  "ds_read_b128 v[164:167], v245 offset:12544\n"
  "v_cvt_pk_bf16_f32 v130, v156, v157\n"
  "v_mfma_f32_16x16x32_bf16 v[136:139], a[124:127], v[64:67], v[136:139]\n"
  "ds_read_b128 v[168:171], v252 offset:4096\n"
  "v_cvt_pk_bf16_f32 v131, v158, v159\n"
  "v_mfma_f32_16x16x32_bf16 v[72:75], a[80:83], v[124:127], v[72:75]\n"
  "v_cvt_pk_bf16_f32 v238, v132, v133\n"
  "v_cvt_pk_bf16_f32 v239, v134, v135\n"
  "v_mfma_f32_16x16x32_bf16 v[116:119], a[104:107], v[124:127], v[116:119]\n"
  "s_waitcnt lgkmcnt(5)\n"
  "v_mfma_f32_16x16x32_bf16 v[136:139], a[128:131], v[124:127], v[136:139]\n"
  "s_waitcnt lgkmcnt(4)\n"
  "ds_read_b128 v[64:67], v252 offset:5120\n"
  "v_mad_u32_u24 v240, v240, s42, v242\n"
  "v_mfma_f32_16x16x32_bf16 v[72:75], a[84:87], v[152:155], v[72:75]\n"
  "s_waitcnt lgkmcnt(4)\n"
  "buffer_store_dwordx4 v[120:123], v240, s[24:27], s41 offen offset:0 sc1\n"
  "v_mfma_f32_16x16x32_bf16 v[116:119], a[108:111], v[152:155], v[116:119]\n"
  "s_waitcnt lgkmcnt(1)\n"
  "buffer_store_dwordx4 v[160:163], v240, s[24:27], s41 offen offset:128 sc1\n"
  "v_mfma_f32_16x16x32_bf16 v[136:139], a[132:135], v[152:155], v[136:139]\n"
  "s_waitcnt lgkmcnt(0)\n"
  "ds_read_b128 v[120:123], v252 offset:6144\n"
  "buffer_store_dwordx4 v[164:167], v240, s[24:27], s41 offen offset:256 sc1\n"
  "v_mfma_f32_16x16x32_bf16 v[72:75], a[88:91], v[168:171], v[72:75]\n"
  "v_mfma_f32_16x16x32_bf16 v[116:119], a[112:115], v[168:171], v[116:119]\n"
  "v_mfma_f32_16x16x32_bf16 v[136:139], a[136:139], v[168:171], v[136:139]\n"
  "s_waitcnt lgkmcnt(0)\n"
  "ds_read_b128 v[124:127], v252 offset:7168\n"
  "v_mfma_f32_16x16x32_bf16 v[72:75], a[92:95], v[64:67], v[72:75]\n"
  "s_nop 0\n"
  "s_nop 0\n"
  "v_mfma_f32_16x16x32_bf16 v[116:119], a[116:119], v[64:67], v[116:119]\n"
  "s_nop 0\n"
  "s_nop 0\n"
  "v_mfma_f32_16x16x32_bf16 v[136:139], a[140:143], v[64:67], v[136:139]\n"
  "ds_read_b128 v[132:135], v252 offset:8192\n"
  "s_nop 0\n"
  "v_mfma_f32_16x16x32_bf16 v[64:67], a[72:75], v[120:123], 0\n"
  "ds_read_u16 v240, v243 offset:16\n"
  "ds_read_b128 v[152:155], v245 offset:0\n"
  "v_mfma_f32_16x16x32_bf16 v[156:159], a[96:99], v[120:123], 0\n"
  "s_waitcnt lgkmcnt(3)\n"
  "ds_read_b128 v[160:163], v245 offset:128\n"
  "ds_read_b128 v[164:167], v245 offset:256\n"
  "v_mfma_f32_16x16x32_bf16 v[168:171], a[120:123], v[120:123], 0\n"
  "ds_read_b128 v[172:175], v252 offset:9216\n"
  "v_cvt_pk_bf16_f32 v72, v72, v73\n"
  "v_mfma_f32_16x16x32_bf16 v[64:67], a[76:79], v[124:127], v[64:67]\n"
  "v_cvt_pk_bf16_f32 v73, v74, v75\n"
  "v_cvt_pk_bf16_f32 v74, v116, v117\n"
  "v_mfma_f32_16x16x32_bf16 v[156:159], a[100:103], v[124:127], v[156:159]\n"
  "s_waitcnt lgkmcnt(5)\n"
  "v_cvt_pk_bf16_f32 v75, v118, v119\n"
  "v_cvt_pk_bf16_f32 v236, v136, v137\n"
  "v_mfma_f32_16x16x32_bf16 v[168:171], a[124:127], v[124:127], v[168:171]\n"
  "s_waitcnt lgkmcnt(4)\n"
  "ds_read_b128 v[116:119], v252 offset:10240\n"
  "v_mad_u32_u24 v240, v240, s42, v242\n"
  "v_mfma_f32_16x16x32_bf16 v[64:67], a[80:83], v[132:135], v[64:67]\n"
  "s_waitcnt lgkmcnt(4)\n"
  "buffer_store_dwordx4 v[152:155], v240, s[24:27], s41 offen offset:0 sc1\n"
  "v_mfma_f32_16x16x32_bf16 v[156:159], a[104:107], v[132:135], v[156:159]\n"
  "s_waitcnt lgkmcnt(1)\n"
  "buffer_store_dwordx4 v[160:163], v240, s[24:27], s41 offen offset:128 sc1\n"
  "v_mfma_f32_16x16x32_bf16 v[168:171], a[128:131], v[132:135], v[168:171]\n"
  "s_waitcnt lgkmcnt(0)\n"
  "ds_read_b128 v[120:123], v252 offset:11264\n"
  "buffer_store_dwordx4 v[164:167], v240, s[24:27], s41 offen offset:256 sc1\n"
  "v_mfma_f32_16x16x32_bf16 v[64:67], a[84:87], v[172:175], v[64:67]\n"
  "v_cvt_pk_bf16_f32 v237, v138, v139\n"
  "v_mfma_f32_16x16x32_bf16 v[156:159], a[108:111], v[172:175], v[156:159]\n"
  "v_mfma_f32_16x16x32_bf16 v[168:171], a[132:135], v[172:175], v[168:171]\n"
  "s_waitcnt lgkmcnt(0)\n"
  "v_mfma_f32_16x16x32_bf16 v[64:67], a[88:91], v[116:119], v[64:67]\n"
  "v_mfma_f32_16x16x32_bf16 v[156:159], a[112:115], v[116:119], v[156:159]\n"
  "v_mfma_f32_16x16x32_bf16 v[168:171], a[136:139], v[116:119], v[168:171]\n"
  "v_mfma_f32_16x16x32_bf16 v[64:67], a[92:95], v[120:123], v[64:67]\n"
  "s_nop 0\n"
  "s_nop 0\n"
  "v_mfma_f32_16x16x32_bf16 v[156:159], a[116:119], v[120:123], v[156:159]\n"
  "s_nop 0\n"
  "s_nop 0\n"
  "v_mfma_f32_16x16x32_bf16 v[168:171], a[140:143], v[120:123], v[168:171]\n"
  "s_nop 0\n"
  "s_nop 0\n"
  "v_mfma_f32_16x16x32_bf16 v[116:119], v[48:51], v[52:55], v[10:13]\n"
  "v_cvt_pk_bf16_f32 v64, v64, v65\n"
  "v_cvt_pk_bf16_f32 v65, v66, v67\n"
  "v_mfma_f32_16x16x32_bf16 v[120:123], v[68:71], v[52:55], 0\n"
  "v_cvt_pk_bf16_f32 v66, v156, v157\n"
  "v_cvt_pk_bf16_f32 v67, v158, v159\n"
  "v_mfma_f32_16x16x32_bf16 v[124:127], v[72:75], v[52:55], 0\n"
  "v_cvt_pk_bf16_f32 v234, v168, v169\n"
  "v_cvt_pk_bf16_f32 v235, v170, v171\n"
  "v_mfma_f32_16x16x32_bf16 v[132:135], v[64:67], v[52:55], v[8:11]\n"
  "s_nop 0\n"
  "s_nop 0\n"
  "v_mfma_f32_16x16x16_bf16 v[116:119], v[232:233], v[14:15], v[116:119]\n"
  "s_nop 0\n"
  "s_nop 0\n"
  "v_mfma_f32_16x16x16_bf16 v[120:123], v[254:255], v[14:15], v[120:123]\n"
  "v_mfma_f32_16x16x16_bf16 v[124:127], v[236:237], v[14:15], v[124:127]\n"
  "v_mfma_f32_16x16x16_bf16 v[132:135], v[234:235], v[14:15], v[132:135]\n"
  "s_nop 0\n"
  "s_nop 0\n"
  "v_mfma_f32_16x16x32_bf16 v[52:55], v[48:51], v[128:131], v[10:13]\n"
  "s_nop 0\n"
  "s_nop 0\n"
  "v_mfma_f32_16x16x32_bf16 v[136:139], v[68:71], v[128:131], 0\n"
  "ds_read_b128 v[152:155], v252 offset:0\n"
  "s_nop 0\n"
  "v_mfma_f32_16x16x32_bf16 v[156:159], v[72:75], v[128:131], 0\n"
  "v_max3_f32 v240, v132, v116, v117\n"
  "v_max3_f32 v240, v240, v118, v119\n"
  "v_mfma_f32_16x16x32_bf16 v[160:163], v[64:67], v[128:131], v[8:11]\n"
  "v_max3_f32 v240, v240, v120, v121\n"
  "v_max3_f32 v240, v240, v122, v123\n"
  "v_mfma_f32_16x16x16_bf16 v[52:55], v[232:233], v[238:239], v[52:55]\n"
  "ds_read_b128 v[128:131], v252 offset:1024\n"
  "v_max3_f32 v240, v240, v124, v125\n"
  "v_mfma_f32_16x16x16_bf16 v[136:139], v[254:255], v[238:239], v[136:139]\n"
  "v_max3_f32 v240, v240, v126, v127\n"
  "v_mfma_f32_16x16x16_bf16 v[156:159], v[236:237], v[238:239], v[156:159]\n"
  "s_waitcnt lgkmcnt(1)\n"
  "v_mfma_f32_16x16x16_bf16 v[160:163], v[234:235], v[238:239], v[160:163]\n"
  "ds_read_b128 v[164:167], v252 offset:2048\n"
  "s_nop 0\n"
  "v_mfma_f32_16x16x32_bf16 v[168:171], a[0:3], v[152:155], 0\n"
  "s_nop 0\n"
  "s_nop 0\n"
  "v_mfma_f32_16x16x32_bf16 v[172:175], a[24:27], v[152:155], 0\n"
  "s_waitcnt lgkmcnt(1)\n"
  "s_nop 0\n"
  "v_max3_f32 v239, v160, v52, v53\n"
  "v_mfma_f32_16x16x32_bf16 v[184:187], a[48:51], v[152:155], 0\n"
  "ds_read_b128 v[188:191], v252 offset:3072\n"
  "v_max3_f32 v239, v239, v54, v55\n"
  "v_mfma_f32_16x16x32_bf16 v[168:171], a[4:7], v[128:131], v[168:171]\n"
  "v_max3_f32 v239, v239, v136, v137\n"
  "v_max3_f32 v239, v239, v138, v139\n"
  "v_mfma_f32_16x16x32_bf16 v[172:175], a[28:31], v[128:131], v[172:175]\n"
  "s_waitcnt lgkmcnt(1)\n"
  "v_max3_f32 v239, v239, v156, v157\n"
  "v_max3_f32 v239, v239, v158, v159\n"
  "v_mfma_f32_16x16x32_bf16 v[184:187], a[52:55], v[128:131], v[184:187]\n"
  "ds_read_b128 v[152:155], v252 offset:4096\n"
  "v_mfma_f32_16x16x32_bf16 v[168:171], a[8:11], v[164:167], v[168:171]\n"
  "v_permlane16_swap_b32 v240, v239\n"
  "v_mfma_f32_16x16x32_bf16 v[172:175], a[32:35], v[164:167], v[172:175]\n"
  "s_waitcnt lgkmcnt(1)\n"
  "v_max_f32 v240, v240, v239\n"
  "v_mov_b32 v239, v240\n"
  "v_mfma_f32_16x16x32_bf16 v[184:187], a[56:59], v[164:167], v[184:187]\n"
  "ds_read_b128 v[128:131], v252 offset:5120\n"
  "v_mfma_f32_16x16x32_bf16 v[168:171], a[12:15], v[188:191], v[168:171]\n"
  "v_permlane32_swap_b32 v240, v239\n"
  "v_mfma_f32_16x16x32_bf16 v[172:175], a[36:39], v[188:191], v[172:175]\n"
  "s_waitcnt lgkmcnt(1)\n"
  "v_max_f32 v240, v240, v239\n"
  "v_mul_f32 v240, 0xbe553b95, v240\n"
  "v_mfma_f32_16x16x32_bf16 v[184:187], a[60:63], v[188:191], v[184:187]\n"
  "ds_read_b128 v[164:167], v252 offset:6144\n"
  "v_mov_b32 v239, v240\n"
  "v_mfma_f32_16x16x32_bf16 v[168:171], a[16:19], v[152:155], v[168:171]\n"
  "s_nop 0\n"
  "v_mfma_f32_16x16x32_bf16 v[172:175], a[40:43], v[152:155], v[172:175]\n"
  "s_waitcnt lgkmcnt(1)\n"
  "v_permlane16_swap_b32 v240, v239\n"
  "v_mfma_f32_16x16x32_bf16 v[184:187], a[64:67], v[152:155], v[184:187]\n"
  "ds_read_b128 v[188:191], v252 offset:7168\n"
  "v_fmamk_f32 v120, v120, 0x3e553b95, v240\n"
  "v_mfma_f32_16x16x32_bf16 v[168:171], a[20:23], v[128:131], v[168:171]\n"
  "v_exp_f32 v120, v120\n"
  "v_mfma_f32_16x16x32_bf16 v[172:175], a[44:47], v[128:131], v[172:175]\n"
  "s_waitcnt lgkmcnt(1)\n"
  "v_fmamk_f32 v121, v121, 0x3e553b95, v240\n"
  "v_fmamk_f32 v122, v122, 0x3e553b95, v240\n"
  "v_mfma_f32_16x16x32_bf16 v[184:187], a[68:71], v[128:131], v[184:187]\n"
  "ds_read_b128 v[152:155], v252 offset:8192\n"
  "v_fmamk_f32 v123, v123, 0x3e553b95, v240\n"
  "v_mfma_f32_16x16x32_bf16 v[128:131], a[0:3], v[164:167], 0\n"
  "v_cvt_pk_bf16_f32 v168, v168, v169\n"
  "v_cvt_pk_bf16_f32 v169, v170, v171\n"
  "v_mfma_f32_16x16x32_bf16 v[192:195], a[24:27], v[164:167], 0\n"
  "s_waitcnt lgkmcnt(1)\n"
  "v_cvt_pk_bf16_f32 v170, v172, v173\n"
  "v_cvt_pk_bf16_f32 v171, v174, v175\n"
  "v_mfma_f32_16x16x32_bf16 v[196:199], a[48:51], v[164:167], 0\n"
  "ds_read_b128 v[200:203], v252 offset:9216\n"
  "v_cvt_pk_bf16_f32 v230, v184, v185\n"
  "v_mfma_f32_16x16x32_bf16 v[128:131], a[4:7], v[188:191], v[128:131]\n"
  "v_cvt_pk_bf16_f32 v231, v186, v187\n"
  "v_fmamk_f32 v116, v116, 0x3e553b95, v240\n"
  "v_mfma_f32_16x16x32_bf16 v[192:195], a[28:31], v[188:191], v[192:195]\n"
  "s_waitcnt lgkmcnt(1)\n"
  "v_exp_f32 v121, v121\n"
  "v_mfma_f32_16x16x32_bf16 v[196:199], a[52:55], v[188:191], v[196:199]\n"
  "ds_read_b128 v[164:167], v252 offset:10240\n"
  "v_cvt_pk_bf16_f32 v120, v120, v121\n"
  "v_mfma_f32_16x16x32_bf16 v[128:131], a[8:11], v[152:155], v[128:131]\n"
  "v_exp_f32 v122, v122\n"
  "v_mfma_f32_16x16x32_bf16 v[192:195], a[32:35], v[152:155], v[192:195]\n"
  "s_waitcnt lgkmcnt(1)\n"
  "v_exp_f32 v123, v123\n"
  "v_mfma_f32_16x16x32_bf16 v[196:199], a[56:59], v[152:155], v[196:199]\n"
  "ds_read_b128 v[172:175], v252 offset:11264\n"
  "v_cvt_pk_bf16_f32 v121, v122, v123\n"
  "v_mfma_f32_16x16x32_bf16 v[128:131], a[12:15], v[200:203], v[128:131]\n"
  "v_exp_f32 v116, v116\n"
  "v_mfma_f32_16x16x32_bf16 v[192:195], a[36:39], v[200:203], v[192:195]\n"
  "s_waitcnt lgkmcnt(1)\n"
  "v_fmamk_f32 v117, v117, 0x3e553b95, v240\n"
  "v_fmamk_f32 v118, v118, 0x3e553b95, v240\n"
  "v_mfma_f32_16x16x32_bf16 v[196:199], a[60:63], v[200:203], v[196:199]\n"
  "v_exp_f32 v117, v117\n"
  "v_mfma_f32_16x16x32_bf16 v[128:131], a[16:19], v[164:167], v[128:131]\n"
  "v_exp_f32 v118, v118\n"
  "v_mfma_f32_16x16x32_bf16 v[192:195], a[40:43], v[164:167], v[192:195]\n"
  "s_waitcnt lgkmcnt(0)\n"
  "v_cvt_pk_bf16_f32 v122, v116, v117\n"
  "v_fmamk_f32 v119, v119, 0x3e553b95, v240\n"
  "v_mfma_f32_16x16x32_bf16 v[196:199], a[64:67], v[164:167], v[196:199]\n"
  "v_exp_f32 v119, v119\n"
  "v_mfma_f32_16x16x32_bf16 v[128:131], a[20:23], v[172:175], v[128:131]\n"
  "v_cvt_pk_bf16_f32 v123, v118, v119\n"
  "v_fmamk_f32 v238, v132, 0x3e553b95, v240\n"
  "v_mfma_f32_16x16x32_bf16 v[192:195], a[44:47], v[172:175], v[192:195]\n"
  "v_exp_f32 v238, v238\n"
  "v_mfma_f32_16x16x32_bf16 v[196:199], a[68:71], v[172:175], v[196:199]\n"
  "v_fmamk_f32 v124, v124, 0x3e553b95, v240\n"
  "v_fmamk_f32 v125, v125, 0x3e553b95, v240\n"
  "v_mfma_f32_16x16x32_bf16 v[116:119], v[48:51], v[168:171], v[10:13]\n"
  "v_cvt_pk_bf16_f32 v128, v128, v129\n"
  "v_cvt_pk_bf16_f32 v129, v130, v131\n"
  "v_mfma_f32_16x16x32_bf16 v[132:135], v[68:71], v[168:171], 0\n"
  "v_cvt_pk_bf16_f32 v130, v192, v193\n"
  "v_cvt_pk_bf16_f32 v131, v194, v195\n"
  "v_mfma_f32_16x16x32_bf16 v[152:155], v[72:75], v[168:171], 0\n"
  "v_cvt_pk_bf16_f32 v14, v196, v197\n"
  "v_cvt_pk_bf16_f32 v15, v198, v199\n"
  "v_mfma_f32_16x16x32_bf16 v[164:167], v[64:67], v[168:171], v[8:11]\n"
  "v_exp_f32 v124, v124\n"
  "v_mfma_f32_16x16x16_bf16 v[116:119], v[232:233], v[230:231], v[116:119]\n"
  "v_exp_f32 v125, v125\n"
  "v_mfma_f32_16x16x16_bf16 v[132:135], v[254:255], v[230:231], v[132:135]\n"
  "v_cvt_pk_bf16_f32 v124, v124, v125\n"
  "v_fmamk_f32 v126, v126, 0x3e553b95, v240\n"
  "v_mfma_f32_16x16x16_bf16 v[152:155], v[236:237], v[230:231], v[152:155]\n"
  "v_exp_f32 v126, v126\n"
  "v_mfma_f32_16x16x16_bf16 v[164:167], v[234:235], v[230:231], v[164:167]\n"
  "v_fmamk_f32 v127, v127, 0x3e553b95, v240\n"
  "v_fmamk_f32 v136, v136, 0x3e553b95, v239\n"
  "v_mfma_f32_16x16x32_bf16 v[168:171], v[48:51], v[128:131], v[10:13]\n"
  "v_exp_f32 v127, v127\n"
  "v_mfma_f32_16x16x32_bf16 v[172:175], v[68:71], v[128:131], 0\n"
  "ds_read_b128 v[48:51], v252 offset:18432\n"
  "v_cvt_pk_bf16_f32 v125, v126, v127\n"
  "v_mfma_f32_16x16x32_bf16 v[68:71], v[72:75], v[128:131], 0\n"
  "v_exp_f32 v136, v136\n"
  "v_mfma_f32_16x16x32_bf16 v[184:187], v[64:67], v[128:131], v[8:11]\n"
  "v_cvt_pk_bf16_f32 v126, v238, 0\n"
  "v_mov_b32 v127, 0\n"
  "v_mfma_f32_16x16x16_bf16 v[168:171], v[232:233], v[14:15], v[168:171]\n"
  "ds_read_b128 v[72:75], v252 offset:19456\n"
  "v_fmamk_f32 v137, v137, 0x3e553b95, v239\n"
  "v_mfma_f32_16x16x16_bf16 v[172:175], v[254:255], v[14:15], v[172:175]\n"
  "v_exp_f32 v137, v137\n"
  "v_mfma_f32_16x16x16_bf16 v[68:71], v[236:237], v[14:15], v[68:71]\n"
  "s_waitcnt lgkmcnt(1)\n"
  "v_cvt_pk_bf16_f32 v136, v136, v137\n"
  "v_fmamk_f32 v138, v138, 0x3e553b95, v239\n"
  "v_mfma_f32_16x16x16_bf16 v[184:187], v[234:235], v[14:15], v[184:187]\n"
  "ds_read_b128 v[188:191], v252 offset:20480\n"
  "v_fmamk_f32 v139, v139, 0x3e553b95, v239\n"
  "v_mfma_f32_16x16x32_bf16 v[192:195], v[48:51], a[144:147], 0\n"
  "v_exp_f32 v138, v138\n"
  "v_mfma_f32_16x16x32_bf16 v[196:199], v[48:51], a[168:171], 0\n"
  "s_waitcnt lgkmcnt(1)\n"
  "v_exp_f32 v139, v139\n"
  "v_mfma_f32_16x16x32_bf16 v[200:203], v[48:51], a[192:195], 0\n"
  "ds_read_b128 v[204:207], v252 offset:21504\n"
  "v_cvt_pk_bf16_f32 v137, v138, v139\n"
  "v_mfma_f32_16x16x32_bf16 v[192:195], v[72:75], a[148:151], v[192:195]\n"
  "v_fmamk_f32 v52, v52, 0x3e553b95, v239\n"
  "v_fmamk_f32 v53, v53, 0x3e553b95, v239\n"
  "v_mfma_f32_16x16x32_bf16 v[196:199], v[72:75], a[172:175], v[196:199]\n"
  "s_waitcnt lgkmcnt(1)\n"
  "v_exp_f32 v52, v52\n"
  "v_mfma_f32_16x16x32_bf16 v[200:203], v[72:75], a[196:199], v[200:203]\n"
  "ds_read_b128 v[48:51], v252 offset:22528\n"
  "v_fmamk_f32 v54, v54, 0x3e553b95, v239\n"
  "v_mfma_f32_16x16x32_bf16 v[192:195], v[188:191], a[152:155], v[192:195]\n"
  "v_exp_f32 v53, v53\n"
  "v_mfma_f32_16x16x32_bf16 v[196:199], v[188:191], a[176:179], v[196:199]\n"
  "s_waitcnt lgkmcnt(1)\n"
  "v_exp_f32 v54, v54\n"
  "v_mfma_f32_16x16x32_bf16 v[200:203], v[188:191], a[200:203], v[200:203]\n"
  "ds_read_b128 v[72:75], v252 offset:23552\n"
  "v_cvt_pk_bf16_f32 v138, v52, v53\n"
  "v_mfma_f32_16x16x32_bf16 v[192:195], v[204:207], a[156:159], v[192:195]\n"
  "v_fmamk_f32 v55, v55, 0x3e553b95, v239\n"
  "v_fmamk_f32 v255, v160, 0x3e553b95, v239\n"
  "v_mfma_f32_16x16x32_bf16 v[196:199], v[204:207], a[180:183], v[196:199]\n"
  "s_waitcnt lgkmcnt(1)\n"
  "v_exp_f32 v55, v55\n"
  "v_mfma_f32_16x16x32_bf16 v[200:203], v[204:207], a[204:207], v[200:203]\n"
  "ds_read_b128 v[160:163], v252 offset:0\n"
  "v_cvt_pk_bf16_f32 v139, v54, v55\n"
  "v_mfma_f32_16x16x32_bf16 v[192:195], v[48:51], a[160:163], v[192:195]\n"
  "v_exp_f32 v255, v255\n"
  "v_mfma_f32_16x16x32_bf16 v[196:199], v[48:51], a[184:187], v[196:199]\n"
  "s_waitcnt lgkmcnt(1)\n"
  "v_fmamk_f32 v156, v156, 0x3e553b95, v239\n"
  "v_fmamk_f32 v157, v157, 0x3e553b95, v239\n"
  "v_mfma_f32_16x16x32_bf16 v[200:203], v[48:51], a[208:211], v[200:203]\n"
  "ds_read_b128 v[52:55], v252 offset:1024\n"
  "v_fmamk_f32 v158, v158, 0x3e553b95, v239\n"
  "v_mfma_f32_16x16x32_bf16 v[192:195], v[72:75], a[164:167], v[192:195]\n"
  "v_exp_f32 v156, v156\n"
  "v_mfma_f32_16x16x32_bf16 v[196:199], v[72:75], a[188:191], v[196:199]\n"
  "s_waitcnt lgkmcnt(1)\n"
  "v_exp_f32 v157, v157\n"
  "v_mfma_f32_16x16x32_bf16 v[200:203], v[72:75], a[212:215], v[200:203]\n"
  "ds_read_b128 v[48:51], v252 offset:2048\n"
  "v_cvt_pk_bf16_f32 v156, v156, v157\n"
  "v_mfma_f32_16x16x32_bf16 v[72:75], v[160:163], a[144:147], 0\n"
  "v_cvt_pk_bf16_f32 v60, v192, v193\n"
  "v_cvt_pk_bf16_f32 v61, v194, v195\n"
  "v_mfma_f32_16x16x32_bf16 v[188:191], v[160:163], a[168:171], 0\n"
  "s_waitcnt lgkmcnt(1)\n"
  "v_cvt_pk_bf16_f32 v112, v196, v197\n"
  "v_cvt_pk_bf16_f32 v113, v198, v199\n"
  "v_mfma_f32_16x16x32_bf16 v[192:195], v[160:163], a[192:195], 0\n"
  "ds_read_b128 v[204:207], v252 offset:3072\n"
  "v_cvt_pk_bf16_f32 v140, v200, v201\n"
  "v_mfma_f32_16x16x32_bf16 v[72:75], v[52:55], a[148:151], v[72:75]\n"
  "v_cvt_pk_bf16_f32 v141, v202, v203\n"
  "v_fmamk_f32 v159, v159, 0x3e553b95, v239\n"
  "v_mfma_f32_16x16x32_bf16 v[188:191], v[52:55], a[172:175], v[188:191]\n"
  "s_waitcnt lgkmcnt(1)\n"
  "v_exp_f32 v158, v158\n"
  "v_mfma_f32_16x16x32_bf16 v[192:195], v[52:55], a[196:199], v[192:195]\n"
  "ds_read_b128 v[160:163], v252 offset:4096\n"
  "v_max3_f32 v254, v164, v116, v117\n"
  "v_mfma_f32_16x16x32_bf16 v[72:75], v[48:51], a[152:155], v[72:75]\n"
  "v_exp_f32 v159, v159\n"
  "v_mfma_f32_16x16x32_bf16 v[188:191], v[48:51], a[176:179], v[188:191]\n"
  "s_waitcnt lgkmcnt(1)\n"
  "v_cvt_pk_bf16_f32 v157, v158, v159\n"
  "v_cvt_pk_bf16_f32 v158, v255, 0\n"
  "v_mfma_f32_16x16x32_bf16 v[192:195], v[48:51], a[200:203], v[192:195]\n"
  "ds_read_b128 v[52:55], v252 offset:5120\n"
  "v_mov_b32 v159, 0\n"
  "v_mfma_f32_16x16x32_bf16 v[72:75], v[204:207], a[156:159], v[72:75]\n"
  "v_max3_f32 v254, v254, v118, v119\n"
  "v_max3_f32 v254, v254, v132, v133\n"
  "v_mfma_f32_16x16x32_bf16 v[188:191], v[204:207], a[180:183], v[188:191]\n"
  "s_waitcnt lgkmcnt(1)\n"
  "v_max3_f32 v254, v254, v134, v135\n"
  "v_max3_f32 v254, v254, v152, v153\n"
  "v_mfma_f32_16x16x32_bf16 v[192:195], v[204:207], a[204:207], v[192:195]\n"
  "ds_read_b128 v[48:51], v252 offset:6144\n"
  "v_max3_f32 v254, v254, v154, v155\n"
  "v_mfma_f32_16x16x32_bf16 v[72:75], v[160:163], a[160:163], v[72:75]\n"
  "v_max3_f32 v255, v184, v168, v169\n"
  "v_max3_f32 v255, v255, v170, v171\n"
  "v_mfma_f32_16x16x32_bf16 v[188:191], v[160:163], a[184:187], v[188:191]\n"
  "s_waitcnt lgkmcnt(1)\n"
  "v_max3_f32 v255, v255, v172, v173\n"
  "v_max3_f32 v255, v255, v174, v175\n"
  "v_mfma_f32_16x16x32_bf16 v[192:195], v[160:163], a[208:211], v[192:195]\n"
  "ds_read_b128 v[196:199], v252 offset:7168\n"
  "v_max3_f32 v255, v255, v68, v69\n"
  "v_mfma_f32_16x16x32_bf16 v[72:75], v[52:55], a[164:167], v[72:75]\n"
  "v_max3_f32 v255, v255, v70, v71\n"
  "v_xor_b32 v10, 0xff800000, v8\n"
  "v_mfma_f32_16x16x32_bf16 v[188:191], v[52:55], a[188:191], v[188:191]\n"
  "s_waitcnt lgkmcnt(1)\n"
  "v_permlane16_swap_b32 v254, v255\n"
  "v_mfma_f32_16x16x32_bf16 v[192:195], v[52:55], a[212:215], v[192:195]\n"
  "ds_read_b128 v[160:163], v252 offset:8192\n"
  "v_max_f32 v254, v254, v255\n"
  "v_mfma_f32_16x16x32_bf16 v[52:55], v[48:51], a[144:147], 0\n"
  "v_cvt_pk_bf16_f32 v200, v72, v73\n"
  "v_cvt_pk_bf16_f32 v201, v74, v75\n"
  "v_mfma_f32_16x16x32_bf16 v[204:207], v[48:51], a[168:171], 0\n"
  "s_waitcnt lgkmcnt(1)\n"
  "v_cvt_pk_bf16_f32 v72, v188, v189\n"
  "v_cvt_pk_bf16_f32 v73, v190, v191\n"
  "v_mfma_f32_16x16x32_bf16 v[208:211], v[48:51], a[192:195], 0\n"
  "ds_read_b128 v[216:219], v252 offset:9216\n"
  "v_cvt_pk_bf16_f32 v188, v192, v193\n"
  "v_mfma_f32_16x16x32_bf16 v[52:55], v[196:199], a[148:151], v[52:55]\n"
  "v_cvt_pk_bf16_f32 v189, v194, v195\n"
  "v_mov_b32 v255, v254\n"
  "v_mfma_f32_16x16x32_bf16 v[204:207], v[196:199], a[172:175], v[204:207]\n"
  "s_waitcnt lgkmcnt(1)\n"
  "v_permlane32_swap_b32 v254, v255\n"
  "v_mfma_f32_16x16x32_bf16 v[208:211], v[196:199], a[196:199], v[208:211]\n"
  "ds_read_b128 v[48:51], v252 offset:10240\n"
  "v_max_f32 v254, v254, v255\n"
  "v_mfma_f32_16x16x32_bf16 v[52:55], v[160:163], a[152:155], v[52:55]\n"
  "v_mul_f32 v254, 0xbe553b95, v254\n"
  "v_mov_b32 v255, v254\n"
  "v_mfma_f32_16x16x32_bf16 v[204:207], v[160:163], a[176:179], v[204:207]\n"
  "s_waitcnt lgkmcnt(1)\n"
  "v_permlane16_swap_b32 v254, v255\n"
  "v_mfma_f32_16x16x32_bf16 v[208:211], v[160:163], a[200:203], v[208:211]\n"
  "ds_read_b128 v[192:195], v252 offset:11264\n"
  "v_fmamk_f32 v132, v132, 0x3e553b95, v254\n"
  "v_mfma_f32_16x16x32_bf16 v[52:55], v[216:219], a[156:159], v[52:55]\n"
  "v_exp_f32 v132, v132\n"
  "v_mfma_f32_16x16x32_bf16 v[204:207], v[216:219], a[180:183], v[204:207]\n"
  "s_waitcnt lgkmcnt(1)\n"
  "v_fmamk_f32 v133, v133, 0x3e553b95, v254\n"
  "v_fmamk_f32 v134, v134, 0x3e553b95, v254\n"
  "v_mfma_f32_16x16x32_bf16 v[208:211], v[216:219], a[204:207], v[208:211]\n"
  "v_exp_f32 v133, v133\n"
  "v_mfma_f32_16x16x32_bf16 v[52:55], v[48:51], a[160:163], v[52:55]\n"
  "v_exp_f32 v134, v134\n"
  "v_mfma_f32_16x16x32_bf16 v[204:207], v[48:51], a[184:187], v[204:207]\n"
  "s_waitcnt lgkmcnt(0)\n"
  "v_cvt_pk_bf16_f32 v132, v132, v133\n"
  "v_fmamk_f32 v135, v135, 0x3e553b95, v254\n"
  "v_mfma_f32_16x16x32_bf16 v[208:211], v[48:51], a[208:211], v[208:211]\n"
  "v_exp_f32 v135, v135\n"
  "v_mfma_f32_16x16x32_bf16 v[52:55], v[192:195], a[164:167], v[52:55]\n"
  "v_cvt_pk_bf16_f32 v133, v134, v135\n"
  "v_fmamk_f32 v116, v116, 0x3e553b95, v254\n"
  "v_mfma_f32_16x16x32_bf16 v[204:207], v[192:195], a[188:191], v[204:207]\n"
  "v_exp_f32 v116, v116\n"
  "v_mfma_f32_16x16x32_bf16 v[208:211], v[192:195], a[212:215], v[208:211]\n"
  "ds_read_u16 v240, v253 offset:2308\n"
  "v_fmamk_f32 v117, v117, 0x3e553b95, v254\n"
  "v_mfma_f32_16x16x32_bf16 v[48:51], v[16:19], v[120:123], 0\n"
  "v_cvt_pk_bf16_f32 v202, v52, v53\n"
  "v_cvt_pk_bf16_f32 v203, v54, v55\n"
  "v_mfma_f32_16x16x32_bf16 v[160:163], v[60:63], v[120:123], 0\n"
  "v_cvt_pk_bf16_f32 v74, v204, v205\n"
  "v_cvt_pk_bf16_f32 v75, v206, v207\n"
  "v_mfma_f32_16x16x32_bf16 v[52:55], v[112:115], v[120:123], 0\n"
  "v_cvt_pk_bf16_f32 v190, v208, v209\n"
  "v_cvt_pk_bf16_f32 v191, v210, v211\n"
  "v_mfma_f32_16x16x32_bf16 v[192:195], v[140:143], v[120:123], 0\n"
  "v_exp_f32 v117, v117\n"
  "v_mfma_f32_16x16x32_bf16 v[48:51], v[16:19], v[124:127], v[48:51]\n"
  "s_waitcnt lgkmcnt(0)\n"
  "v_add_u32 v239, v240, v250\n"
  "v_add_u32 v238, v240, v249\n"
  "v_mfma_f32_16x16x32_bf16 v[160:163], v[200:203], v[124:127], v[160:163]\n"
  "ds_read_u16 v237, v253 offset:2310\n"
  "v_add_u32 v236, v240, v248\n"
  "v_mfma_f32_16x16x32_bf16 v[52:55], v[72:75], v[124:127], v[52:55]\n"
  "v_cvt_pk_bf16_f32 v134, v116, v117\n"
  "v_fmamk_f32 v118, v118, 0x3e553b95, v254\n"
  "v_mfma_f32_16x16x32_bf16 v[192:195], v[188:191], v[124:127], v[192:195]\n"
  "v_rcp_f32 v48, v48\n"
  "v_mfma_f32_16x16x32_bf16 v[120:123], v[16:19], v[136:139], 0\n"
  "v_mul_f32 v160, v160, v48\n"
  "v_mul_f32 v161, v161, v48\n"
  "v_mfma_f32_16x16x32_bf16 v[124:127], v[60:63], v[136:139], 0\n"
  "v_cvt_pk_bf16_f32 v160, v160, v161\n"
  "v_mul_f32 v162, v162, v48\n"
  "v_mfma_f32_16x16x32_bf16 v[196:199], v[112:115], v[136:139], 0\n"
  "s_waitcnt lgkmcnt(0)\n"
  "v_mul_f32 v163, v163, v48\n"
  "v_cvt_pk_bf16_f32 v161, v162, v163\n"
  "v_mfma_f32_16x16x32_bf16 v[204:207], v[140:143], v[136:139], 0\n"
  "v_mul_f32 v52, v52, v48\n"
  "v_mul_f32 v53, v53, v48\n"
  "v_mfma_f32_16x16x32_bf16 v[120:123], v[16:19], v[156:159], v[120:123]\n"
  "ds_read_b128 v[136:139], v239 offset:0\n"
  "v_cvt_pk_bf16_f32 v162, v52, v53\n"
  "v_mfma_f32_16x16x32_bf16 v[124:127], v[200:203], v[156:159], v[124:127]\n"
  "v_mul_f32 v54, v54, v48\n"
  "v_mul_f32 v55, v55, v48\n"
  "v_mfma_f32_16x16x32_bf16 v[196:199], v[72:75], v[156:159], v[196:199]\n"
  "ds_read_b128 v[216:219], v238 offset:0\n"
  "ds_read_b128 v[220:223], v238 offset:3072\n"
  "v_mfma_f32_16x16x32_bf16 v[204:207], v[188:191], v[156:159], v[204:207]\n"
  "v_cvt_pk_bf16_f32 v163, v54, v55\n"
  "v_mul_f32 v192, v192, v48\n"
  "v_mul_f32 v193, v193, v48\n"
  "v_cvt_pk_bf16_f32 v192, v192, v193\n"
  "v_rcp_f32 v120, v120\n"
  "v_mul_f32 v194, v194, v48\n"
  "v_mul_f32 v195, v195, v48\n"
  "s_waitcnt lgkmcnt(2)\n"
  "v_mfma_f32_16x16x32_bf16 v[52:55], v[24:27], v[136:139], 0\n"
  "v_cvt_pk_bf16_f32 v193, v194, v195\n"
  "s_mov_b64 exec, s[58:59]\n"
  "ds_write_b128 v251, v[160:163] offset:39936\n"
  "ds_write_b64 v247, v[192:193] offset:39936\n"
  "s_mov_b64 exec, -1\n"
  "s_waitcnt lgkmcnt(2)\n"
  "v_smfmac_f32_16x16x64_bf16 v[52:55], v[20:23], v[216:223], v9\n"
  "v_mul_f32 v124, v124, v120\n"
  "v_mul_f32 v125, v125, v120\n"
  "v_cvt_pk_bf16_f32 v124, v124, v125\n"
  "v_mul_f32 v126, v126, v120\n"
  "v_mul_f32 v127, v127, v120\n"
  "v_cvt_pk_bf16_f32 v125, v126, v127\n"
  "v_mul_f32 v196, v196, v120\n"
  "v_mul_f32 v197, v197, v120\n"
  "v_cvt_pk_bf16_f32 v126, v196, v197\n"
  "v_mul_f32 v198, v198, v120\n"
  "v_mul_f32 v199, v199, v120\n"
  "v_cvt_pk_bf16_f32 v127, v198, v199\n"
  "ds_write_b128 v251, v[124:127] offset:46080\n"
  "v_mul_f32 v204, v204, v120\n"
  "v_mul_f32 v205, v205, v120\n"
  "v_cvt_pk_bf16_f32 v204, v204, v205\n"
  "v_mul_f32 v206, v206, v120\n"
  "v_mul_f32 v207, v207, v120\n"
  "v_cvt_pk_bf16_f32 v205, v206, v207\n"
  "ds_write_b64 v247, v[204:205] offset:46080\n"
  "v_add_u32 v240, v237, v250\n"
  "v_add_u32 v233, v237, v249\n"
  "v_add_u32 v232, v237, v248\n"
  "v_exp_f32 v118, v118\n"
  "v_fmamk_f32 v119, v119, 0x3e553b95, v254\n"
  "v_exp_f32 v119, v119\n"
  "v_fmamk_f32 v237, v164, 0x3e553b95, v254\n"
  "v_cvt_pk_bf16_f32 v135, v118, v119\n"
  "v_exp_f32 v237, v237\n"
  "v_fmamk_f32 v152, v152, 0x3e553b95, v254\n"
  "v_exp_f32 v152, v152\n"
  "v_fmamk_f32 v153, v153, 0x3e553b95, v254\n"
  "v_exp_f32 v153, v153\n"
  "v_fmamk_f32 v154, v154, 0x3e553b95, v254\n"
  "v_cvt_pk_bf16_f32 v152, v152, v153\n"
  "v_exp_f32 v154, v154\n"
  "s_waitcnt lgkmcnt(0)\n"
  "s_barrier\n"
  "ds_read_b128 v[48:51], v252 offset:39936\n"
  "ds_read_b128 v[116:119], v236 offset:0\n"
  "ds_read_b128 v[120:123], v236 offset:3072\n"
  "v_fmamk_f32 v155, v155, 0x3e553b95, v254\n"
  "v_exp_f32 v155, v155\n"
  "v_fmamk_f32 v172, v172, 0x3e553b95, v255\n"
  "v_cvt_pk_bf16_f32 v153, v154, v155\n"
  "v_cvt_pk_bf16_f32 v154, v237, 0\n"
  "v_mov_b32 v155, 0\n"
  "v_exp_f32 v172, v172\n"
  "v_fmamk_f32 v173, v173, 0x3e553b95, v255\n"
  "v_exp_f32 v173, v173\n"
  "v_fmamk_f32 v174, v174, 0x3e553b95, v255\n"
  "v_exp_f32 v174, v174\n"
  "v_cvt_pk_bf16_f32 v172, v172, v173\n"
  "v_fmamk_f32 v175, v175, 0x3e553b95, v255\n"
  "s_waitcnt lgkmcnt(2)\n"
  "v_mfma_f32_16x16x32_bf16 v[104:107], a[216:219], v[48:51], v[104:107]\n"
  "ds_read_b128 v[124:127], v252 offset:40960\n"
  "ds_read_b128 v[136:139], v240 offset:0\n"
  "v_mfma_f32_16x16x32_bf16 v[108:111], a[232:235], v[48:51], v[108:111]\n"
  "s_waitcnt lgkmcnt(3)\n"
  "ds_read_u16 v254, v253 offset:2312\n"
  "v_fmamk_f32 v168, v168, 0x3e553b95, v255\n"
  "v_mfma_f32_16x16x32_bf16 v[32:35], a[244:247], v[48:51], v[32:35]\n"
  "s_waitcnt lgkmcnt(3)\n"
  "ds_read_b128 v[156:159], v233 offset:0\n"
  "ds_read_b128 v[160:163], v233 offset:3072\n"
  "v_smfmac_f32_16x16x64_bf16 v[52:55], v[28:31], v[116:123], v9\n"
  "ds_read_b128 v[48:51], v252 offset:41984\n"
  "v_exp_f32 v175, v175\n"
  "v_exp_f32 v168, v168\n"
  "v_cvt_pk_bf16_f32 v173, v174, v175\n"
  "v_fmamk_f32 v169, v169, 0x3e553b95, v255\n"
  "v_fmamk_f32 v170, v170, 0x3e553b95, v255\n"
  "s_waitcnt lgkmcnt(5)\n"
  "v_mfma_f32_16x16x32_bf16 v[104:107], a[220:223], v[124:127], v[104:107]\n"
  "ds_read_b128 v[116:119], v232 offset:0\n"
  "ds_read_b128 v[120:123], v232 offset:3072\n"
  "s_waitcnt lgkmcnt(6)\n"
  "v_mfma_f32_16x16x32_bf16 v[164:167], v[24:27], v[136:139], 0\n"
  "s_waitcnt lgkmcnt(5)\n"
  "v_add_u32 v237, v254, v250\n"
  "v_add_u32 v231, v254, v249\n"
  "v_mfma_f32_16x16x32_bf16 v[108:111], a[236:239], v[124:127], v[108:111]\n"
  "s_waitcnt lgkmcnt(4)\n"
  "ds_read_b128 v[136:139], v237 offset:0\n"
  "v_add_u32 v250, v254, v248\n"
  "v_mfma_f32_16x16x32_bf16 v[32:35], a[248:251], v[124:127], v[32:35]\n"
  "s_waitcnt lgkmcnt(4)\n"
  "v_exp_f32 v169, v169\n"
  "v_smfmac_f32_16x16x64_bf16 v[164:167], v[20:23], v[156:163], v9\n"
  "s_waitcnt lgkmcnt(3)\n"
  "ds_read_b128 v[192:195], v231 offset:0\n"
  "ds_read_b128 v[196:199], v231 offset:3072\n"
  "v_mfma_f32_16x16x32_bf16 v[104:107], a[224:227], v[48:51], v[104:107]\n"
  "s_waitcnt lgkmcnt(4)\n"
  "ds_read_b128 v[124:127], v252 offset:43008\n"
  "v_cvt_pk_bf16_f32 v174, v168, v169\n"
  "v_mfma_f32_16x16x32_bf16 v[108:111], a[240:243], v[48:51], v[108:111]\n"
  "s_waitcnt lgkmcnt(4)\n"
  "v_exp_f32 v170, v170\n"
  "v_smfmac_f32_16x16x64_bf16 v[164:167], v[28:31], v[116:123], v9\n"
  "s_waitcnt lgkmcnt(3)\n"
  "ds_read_b128 v[156:159], v250 offset:0\n"
  "ds_read_b128 v[160:163], v250 offset:3072\n"
  "v_mfma_f32_16x16x32_bf16 v[116:119], v[24:27], v[136:139], 0\n"
  "ds_read_b128 v[120:123], v241 offset:8192\n"
  "v_fmamk_f32 v171, v171, 0x3e553b95, v255\n"
  "v_mfma_f32_16x16x32_bf16 v[32:35], a[252:255], v[48:51], v[32:35]\n"
  "s_waitcnt lgkmcnt(5)\n"
  "ds_read_b128 v[24:27], v239 offset:128\n"
  "v_fmamk_f32 v254, v184, 0x3e553b95, v255\n"
  "v_mfma_f32_16x16x32_bf16 v[48:51], v[16:19], v[132:135], 0\n"
  "s_waitcnt lgkmcnt(5)\n"
  "v_exp_f32 v171, v171\n"
  "v_smfmac_f32_16x16x64_bf16 v[116:119], v[20:23], v[192:199], v9\n"
  "s_waitcnt lgkmcnt(4)\n"
  "ds_read_b128 v[136:139], v241 offset:0\n"
  "v_cvt_pk_bf16_f32 v175, v170, v171\n"
  "v_mfma_f32_16x16x32_bf16 v[104:107], a[228:231], v[124:127], v[104:107]\n"
  "s_waitcnt lgkmcnt(4)\n"
  "ds_read_b128 v[192:195], v238 offset:128\n"
  "ds_read_b128 v[196:199], v238 offset:3200\n"
  "v_mfma_f32_16x16x32_bf16 v[108:111], v[4:7], v[124:127], v[108:111]\n"
  "s_waitcnt lgkmcnt(5)\n"
  "ds_read_b128 v[20:23], v241 offset:4096\n"
  "v_fmamk_f32 v68, v68, 0x3e553b95, v255\n"
  "v_smfmac_f32_16x16x64_bf16 v[116:119], v[28:31], v[156:163], v9\n"
  "s_waitcnt lgkmcnt(5)\n"
  "ds_read_b128 v[204:207], v236 offset:128\n"
  "v_fmamk_f32 v69, v69, 0x3e553b95, v255\n"
  "v_mfma_f32_16x16x32_bf16 v[32:35], v[76:79], v[124:127], v[32:35]\n"
  "s_waitcnt lgkmcnt(5)\n"
  "ds_read_b128 v[208:211], v236 offset:3200\n"
  "v_fmamk_f32 v70, v70, 0x3e553b95, v255\n"
  "v_mfma_f32_16x16x32_bf16 v[28:31], v[120:123], v[24:27], 0\n"
  "s_waitcnt lgkmcnt(5)\n"
  "ds_read_b128 v[124:127], v240 offset:128\n"
  "v_fmamk_f32 v71, v71, 0x3e553b95, v255\n"
  "v_mfma_f32_16x16x32_bf16 v[24:27], v[60:63], v[132:135], 0\n"
  "s_waitcnt lgkmcnt(5)\n"
  "v_exp_f32 v254, v254\n"
  "v_mfma_f32_16x16x32_bf16 v[156:159], v[112:115], v[132:135], 0\n"
  "s_waitcnt lgkmcnt(4)\n"
  "v_exp_f32 v68, v68\n"
  "v_smfmac_f32_16x16x64_bf16 v[28:31], v[136:139], v[192:199], v9\n"
  "s_waitcnt lgkmcnt(3)\n"
  "ds_read_b128 v[216:219], v233 offset:128\n"
  "ds_read_b128 v[220:223], v233 offset:3200\n"
  "v_mfma_f32_16x16x32_bf16 v[160:163], v[140:143], v[132:135], 0\n"
  "s_waitcnt lgkmcnt(4)\n"
  "v_exp_f32 v69, v69\n"
  "v_mfma_f32_16x16x32_bf16 v[48:51], v[16:19], v[152:155], v[48:51]\n"
  "s_waitcnt lgkmcnt(3)\n"
  "v_exp_f32 v70, v70\n"
  "v_smfmac_f32_16x16x64_bf16 v[28:31], v[20:23], v[204:211], v9\n"
  "s_waitcnt lgkmcnt(2)\n"
  "ds_read_b128 v[192:195], v232 offset:128\n"
  "ds_read_b128 v[196:199], v232 offset:3200\n"
  "v_mfma_f32_16x16x32_bf16 v[132:135], v[120:123], v[124:127], 0\n"
  "ds_read_b128 v[168:171], v237 offset:128\n"
  "v_cvt_pk_bf16_f32 v68, v68, v69\n"
  "v_mfma_f32_16x16x32_bf16 v[24:27], v[200:203], v[152:155], v[24:27]\n"
  "s_waitcnt lgkmcnt(4)\n"
  "v_exp_f32 v71, v71\n"
  "v_mfma_f32_16x16x32_bf16 v[156:159], v[72:75], v[152:155], v[156:159]\n"
  "s_waitcnt lgkmcnt(3)\n"
  "v_cvt_pk_bf16_f32 v69, v70, v71\n"
  "v_cvt_pk_bf16_f32 v70, v254, 0\n"
  "v_smfmac_f32_16x16x64_bf16 v[132:135], v[136:139], v[216:223], v9\n"
  "ds_read_b128 v[204:207], v231 offset:128\n"
  "ds_read_b128 v[208:211], v231 offset:3200\n"
  "v_mfma_f32_16x16x32_bf16 v[160:163], v[188:191], v[152:155], v[160:163]\n"
  "s_waitcnt lgkmcnt(4)\n"
  "v_mov_b32 v71, 0\n"
  "v_add_f32 v255, v52, v53\n"
  "v_mfma_f32_16x16x32_bf16 v[124:127], v[16:19], v[172:175], 0\n"
  "s_waitcnt lgkmcnt(3)\n"
  "v_mul_f32 v254, v52, v52\n"
  "v_fmac_f32 v254, v53, v53\n"
  "v_smfmac_f32_16x16x64_bf16 v[132:135], v[20:23], v[192:199], v9\n"
  "s_waitcnt lgkmcnt(2)\n"
  "ds_read_b128 v[216:219], v250 offset:128\n"
  "ds_read_b128 v[220:223], v250 offset:3200\n"
  "v_mfma_f32_16x16x32_bf16 v[152:155], v[120:123], v[168:171], 0\n"
  "ds_read_b128 v[184:187], v239 offset:256\n"
  "v_add_f32 v255, v54, v255\n"
  "v_mfma_f32_16x16x32_bf16 v[120:123], v[60:63], v[172:175], 0\n"
  "s_waitcnt lgkmcnt(4)\n"
  "ds_read_b128 v[168:171], v241 offset:20480\n"
  "v_fmac_f32 v254, v54, v54\n"
  "v_mfma_f32_16x16x32_bf16 v[60:63], v[112:115], v[172:175], 0\n"
  "s_waitcnt lgkmcnt(4)\n"
  "v_add_f32 v255, v55, v255\n"
  "v_fmac_f32 v254, v55, v55\n"
  "v_smfmac_f32_16x16x64_bf16 v[152:155], v[136:139], v[204:211], v9\n"
  "ds_read_b128 v[192:195], v238 offset:256\n"
  "ds_read_b128 v[196:199], v238 offset:3328\n"
  "v_mfma_f32_16x16x32_bf16 v[112:115], v[140:143], v[172:175], 0\n"
  "s_waitcnt lgkmcnt(5)\n"
  "ds_read_b128 v[136:139], v241 offset:12288\n"
  "v_add_f32 v255, v28, v255\n"
  "v_mfma_f32_16x16x32_bf16 v[124:127], v[16:19], v[68:71], v[124:127]\n"
  "s_waitcnt lgkmcnt(5)\n"
  "v_fmac_f32 v254, v28, v28\n"
  "v_add_f32 v255, v29, v255\n"
  "v_smfmac_f32_16x16x64_bf16 v[152:155], v[20:23], v[216:223], v9\n"
  "s_waitcnt lgkmcnt(4)\n"
  "ds_read_b128 v[204:207], v236 offset:256\n"
  "ds_read_b128 v[208:211], v236 offset:3328\n"
  "v_mfma_f32_16x16x32_bf16 v[120:123], v[200:203], v[68:71], v[120:123]\n"
  "s_waitcnt lgkmcnt(5)\n"
  "ds_read_b128 v[20:23], v241 offset:16384\n"
  "v_fmac_f32 v254, v29, v29\n"
  "v_mfma_f32_16x16x32_bf16 v[140:143], v[168:171], v[184:187], 0\n"
  "s_waitcnt lgkmcnt(5)\n"
  "ds_read_b128 v[172:175], v240 offset:256\n"
  "v_add_f32 v255, v30, v255\n"
  "v_mfma_f32_16x16x32_bf16 v[60:63], v[72:75], v[68:71], v[60:63]\n"
  "s_waitcnt lgkmcnt(5)\n"
  "v_fmac_f32 v254, v30, v30\n"
  "v_add_f32 v255, v31, v255\n"
  "v_mfma_f32_16x16x32_bf16 v[112:115], v[188:191], v[68:71], v[112:115]\n"
  "s_waitcnt lgkmcnt(4)\n"
  "v_fmac_f32 v254, v31, v31\n"
  "v_add_f32 v249, v164, v165\n"
  "v_smfmac_f32_16x16x64_bf16 v[140:143], v[136:139], v[192:199], v9\n"
  "s_waitcnt lgkmcnt(3)\n"
  "ds_read_b128 v[216:219], v233 offset:256\n"
  "ds_read_b128 v[220:223], v233 offset:3328\n"
  "ds_read_b128 v[68:71], v252 offset:44032\n"
  "v_mul_f32 v248, v164, v164\n"
  "v_fmac_f32 v248, v165, v165\n"
  "v_add_f32 v249, v166, v249\n"
  "v_fmac_f32 v248, v166, v166\n"
  "v_add_f32 v249, v167, v249\n"
  "s_waitcnt lgkmcnt(4)\n"
  "v_smfmac_f32_16x16x64_bf16 v[140:143], v[20:23], v[204:211], v9\n"
  "ds_read_b128 v[192:195], v232 offset:256\n"
  "ds_read_b128 v[196:199], v232 offset:3328\n"
  "s_waitcnt lgkmcnt(5)\n"
  "v_mfma_f32_16x16x32_bf16 v[184:187], v[168:171], v[172:175], 0\n"
  "ds_read_b128 v[204:207], v237 offset:256\n"
  "v_fmac_f32 v248, v167, v167\n"
  "v_add_f32 v249, v132, v249\n"
  "v_fmac_f32 v248, v132, v132\n"
  "v_add_f32 v255, v140, v255\n"
  "v_fmac_f32 v254, v140, v140\n"
  "v_add_f32 v255, v141, v255\n"
  "v_fmac_f32 v254, v141, v141\n"
  "s_waitcnt lgkmcnt(4)\n"
  "v_smfmac_f32_16x16x64_bf16 v[184:187], v[136:139], v[216:223], v9\n"
  "ds_read_b128 v[172:175], v252 offset:45056\n"
  "v_add_f32 v255, v142, v255\n"
  "s_waitcnt lgkmcnt(4)\n"
  "v_mfma_f32_16x16x32_bf16 v[104:107], v[80:83], v[68:71], v[104:107]\n"
  "s_waitcnt lgkmcnt(3)\n"
  "ds_read_b128 v[216:219], v231 offset:256\n"
  "ds_read_b128 v[220:223], v231 offset:3328\n"
  "v_mfma_f32_16x16x32_bf16 v[108:111], v[84:87], v[68:71], v[108:111]\n"
  "s_waitcnt lgkmcnt(4)\n"
  "v_fmac_f32 v254, v142, v142\n"
  "v_add_f32 v255, v143, v255\n"
  "v_smfmac_f32_16x16x64_bf16 v[184:187], v[20:23], v[192:199], v9\n"
  "s_waitcnt lgkmcnt(3)\n"
  "ds_read_b128 v[224:227], v250 offset:256\n"
  "ds_read_b128 v[228:231], v250 offset:3328\n"
  "v_mfma_f32_16x16x32_bf16 v[32:35], v[88:91], v[68:71], v[32:35]\n"
  "ds_read_b128 v[192:195], v252 offset:46080\n"
  "v_fmac_f32 v254, v143, v143\n"
  "v_mfma_f32_16x16x32_bf16 v[68:71], v[168:171], v[204:207], 0\n"
  "s_waitcnt lgkmcnt(5)\n"
  "v_permlane16_swap_b32 v255, v254\n"
  "v_mfma_f32_16x16x32_bf16 v[104:107], v[92:95], v[172:175], v[104:107]\n"
  "s_waitcnt lgkmcnt(4)\n"
  "v_add_f32 v255, v255, v254\n"
  "v_add_f32 v249, v133, v249\n"
  "v_mfma_f32_16x16x32_bf16 v[108:111], v[96:99], v[172:175], v[108:111]\n"
  "s_waitcnt lgkmcnt(3)\n"
  "v_fmac_f32 v248, v133, v133\n"
  "v_add_f32 v249, v134, v249\n"
  "v_smfmac_f32_16x16x64_bf16 v[68:71], v[136:139], v[216:223], v9\n"
  "s_waitcnt lgkmcnt(2)\n"
  "ds_read_b128 v[168:171], v252 offset:47104\n"
  "v_cvt_pk_bf16_f32 v104, v104, v105\n"
  "v_mfma_f32_16x16x32_bf16 v[32:35], v[100:103], v[172:175], v[32:35]\n"
  "s_waitcnt lgkmcnt(2)\n"
  "v_cvt_pk_bf16_f32 v105, v106, v107\n"
  "v_cvt_pk_bf16_f32 v108, v108, v109\n"
  "v_smfmac_f32_16x16x64_bf16 v[68:71], v[20:23], v[224:231], v9\n"
  "s_waitcnt lgkmcnt(1)\n"
  "ds_write_b64 v244, v[104:105] offset:12288\n"
  "v_mfma_f32_16x16x32_bf16 v[144:147], a[216:219], v[192:195], v[144:147]\n"
  "v_cvt_pk_bf16_f32 v109, v110, v111\n"
  "v_cvt_pk_bf16_f32 v32, v32, v33\n"
  "v_mfma_f32_16x16x32_bf16 v[180:183], a[232:235], v[192:195], v[180:183]\n"
  "ds_write_b64 v244, v[108:109] offset:12416\n"
  "v_mfma_f32_16x16x32_bf16 v[176:179], a[244:247], v[192:195], v[176:179]\n"
  "s_waitcnt lgkmcnt(2)\n"
  "v_cvt_pk_bf16_f32 v33, v34, v35\n"
  "v_fmac_f32 v248, v134, v134\n"
  "v_mfma_f32_16x16x32_bf16 v[144:147], a[220:223], v[168:171], v[144:147]\n"
  "ds_write_b64 v244, v[32:33] offset:12544\n"
  "v_mfma_f32_16x16x32_bf16 v[180:183], a[236:239], v[168:171], v[180:183]\n"
  "v_add_f32 v249, v135, v249\n"
  "v_fmac_f32 v248, v135, v135\n"
  "v_mfma_f32_16x16x32_bf16 v[176:179], a[248:251], v[168:171], v[176:179]\n"
  "ds_read_b128 v[20:23], v252 offset:48128\n"
  "ds_read_b128 v[32:35], v252 offset:49152\n"
  "v_add_f32 v249, v184, v249\n"
  "v_fmac_f32 v248, v184, v184\n"
  "v_add_f32 v249, v185, v249\n"
  "v_fmac_f32 v248, v185, v185\n"
  "v_add_f32 v249, v186, v249\n"
  "v_fmac_f32 v248, v186, v186\n"
  "v_add_f32 v249, v187, v249\n"
  "v_fmac_f32 v248, v187, v187\n"
  "v_add_f32 v254, v116, v117\n"
  "v_mul_f32 v250, v116, v116\n"
  "v_permlane16_swap_b32 v249, v248\n"
  "v_add_f32 v249, v249, v248\n"
  "v_fmac_f32 v250, v117, v117\n"
  "v_add_f32 v254, v118, v254\n"
  "v_permlane32_swap_b32 v255, v249\n"
  "v_add_f32 v255, v255, v249\n"
  "s_waitcnt lgkmcnt(1)\n"
  "v_mfma_f32_16x16x32_bf16 v[144:147], a[224:227], v[20:23], v[144:147]\n"
  "ds_write_b32 v246, v255 offset:0\n"
  "v_mfma_f32_16x16x32_bf16 v[180:183], a[240:243], v[20:23], v[180:183]\n"
  "s_waitcnt lgkmcnt(1)\n"
  "v_fmac_f32 v250, v118, v118\n"
  "v_add_f32 v254, v119, v254\n"
  "v_mfma_f32_16x16x32_bf16 v[176:179], a[252:255], v[20:23], v[176:179]\n"
  "ds_read_b128 v[104:107], v252 offset:50176\n"
  "v_fmac_f32 v250, v119, v119\n"
  "v_mfma_f32_16x16x32_bf16 v[144:147], a[228:231], v[32:35], v[144:147]\n"
  "v_add_f32 v254, v152, v254\n"
  "v_fmac_f32 v250, v152, v152\n"
  "v_mfma_f32_16x16x32_bf16 v[180:183], v[4:7], v[32:35], v[180:183]\n"
  "v_add_f32 v254, v153, v254\n"
  "v_fmac_f32 v250, v153, v153\n"
  "v_mfma_f32_16x16x32_bf16 v[176:179], v[76:79], v[32:35], v[176:179]\n"
  "v_add_f32 v254, v154, v254\n"
  "v_fmac_f32 v250, v154, v154\n"
  "v_add_f32 v254, v155, v254\n"
  "v_fmac_f32 v250, v155, v155\n"
  "v_add_f32 v254, v68, v254\n"
  "v_fmac_f32 v250, v68, v68\n"
  "v_add_f32 v254, v69, v254\n"
  "v_fmac_f32 v250, v69, v69\n"
  "s_waitcnt lgkmcnt(0)\n"
  "v_mfma_f32_16x16x32_bf16 v[144:147], v[80:83], v[104:107], v[144:147]\n"
  "v_add_f32 v254, v70, v254\n"
  "v_fmac_f32 v250, v70, v70\n"
  "v_mfma_f32_16x16x32_bf16 v[180:183], v[84:87], v[104:107], v[180:183]\n"
  "v_add_f32 v254, v71, v254\n"
  "v_fmac_f32 v250, v71, v71\n"
  "v_mfma_f32_16x16x32_bf16 v[176:179], v[88:91], v[104:107], v[176:179]\n"
  "v_rcp_f32 v48, v48\n"
  "v_permlane16_swap_b32 v254, v250\n"
  "v_add_f32 v254, v254, v250\n"
  "v_mov_b32 v255, v254\n"
  "v_mul_f32 v24, v24, v48\n"
  "v_mul_f32 v25, v25, v48\n"
  "v_permlane32_swap_b32 v254, v255\n"
  "v_add_f32 v254, v254, v255\n"
  "ds_write_b32 v246, v254 offset:1024\n"
  "v_cvt_pk_bf16_f32 v24, v24, v25\n"
  "v_mul_f32 v26, v26, v48\n"
  "v_mul_f32 v27, v27, v48\n"
  "v_cvt_pk_bf16_f32 v25, v26, v27\n"
  "v_mul_f32 v156, v156, v48\n"
  "v_mul_f32 v157, v157, v48\n"
  "v_cvt_pk_bf16_f32 v26, v156, v157\n"
  "v_mul_f32 v158, v158, v48\n"
  "v_mul_f32 v159, v159, v48\n"
  "v_cvt_pk_bf16_f32 v27, v158, v159\n"
  "v_mul_f32 v160, v160, v48\n"
  "v_mul_f32 v161, v161, v48\n"
  "v_cvt_pk_bf16_f32 v160, v160, v161\n"
  "v_mul_f32 v162, v162, v48\n"
  "v_mul_f32 v163, v163, v48\n"
  "v_cvt_pk_bf16_f32 v161, v162, v163\n"
  "v_rcp_f32 v124, v124\n"
  "s_mov_b64 exec, s[74:75]\n"
  "v_mov_b32 v8, 0\n"
  "s_mov_b64 exec, -1\n"
  "s_waitcnt lgkmcnt(0)\n"
  "s_barrier\n"
  "ds_read_b128 v[20:23], v252 offset:51200\n"
  "ds_write_b128 v251, v[24:27] offset:27648\n"
  "v_mul_f32 v120, v120, v124\n"
  "v_mul_f32 v121, v121, v124\n"
  "ds_write_b64 v247, v[160:161] offset:27648\n"
  "v_cvt_pk_bf16_f32 v120, v120, v121\n"
  "v_mul_f32 v122, v122, v124\n"
  "ds_read_b128 v[24:27], v253 offset:0\n"
  "s_waitcnt lgkmcnt(3)\n"
  "v_mfma_f32_16x16x32_bf16 v[144:147], v[92:95], v[20:23], v[144:147]\n"
  "ds_read_b128 v[32:35], v253 offset:256\n"
  "v_mul_f32 v123, v123, v124\n"
  "v_mfma_f32_16x16x32_bf16 v[180:183], v[96:99], v[20:23], v[180:183]\n"
  "ds_read_b128 v[48:51], v253 offset:512\n"
  "v_cvt_pk_bf16_f32 v121, v122, v123\n"
  "v_mfma_f32_16x16x32_bf16 v[176:179], v[100:103], v[20:23], v[176:179]\n"
  "ds_read_b128 v[104:107], v253 offset:768\n"
  "v_mul_f32 v60, v60, v124\n"
  "v_cvt_pk_bf16_f32 v144, v144, v145\n"
  "v_cvt_pk_bf16_f32 v145, v146, v147\n"
  "ds_write_b64 v244, v[144:145] offset:18432\n"
  "v_cvt_pk_bf16_f32 v180, v180, v181\n"
  "v_cvt_pk_bf16_f32 v181, v182, v183\n"
  "ds_write_b64 v244, v[180:181] offset:18560\n"
  "s_waitcnt lgkmcnt(5)\n"
  "v_add_f32 v24, v24, v25\n"
  "v_add_f32 v26, v26, v27\n"
  "v_add_f32 v24, v24, v26\n"
  "s_waitcnt lgkmcnt(4)\n"
  "v_add_f32 v32, v32, v33\n"
  "v_add_f32 v34, v34, v35\n"
  "v_add_f32 v32, v32, v34\n"
  "v_mul_f32 v24, 0xbbaaaaab, v24\n"
  "v_mul_f32 v32, 0x3baaaaab, v32\n"
  "v_fma_f32 v32, -v24, v24, v32\n"
  "v_add_f32_e64 v32, abs(v32), s43\n"
  "v_rsq_f32 v32, v32\n"
  "v_cvt_pk_bf16_f32 v176, v176, v177\n"
  "v_mul_f32 v24, v24, v32\n"
  "v_fma_f32 v25, v52, v32, v24\n"
  "v_fma_f32 v26, v53, v32, v24\n"
  "v_cvt_pk_bf16_f32 v20, v25, v26\n"
  "v_fma_f32 v33, v54, v32, v24\n"
  "v_fma_f32 v34, v55, v32, v24\n"
  "v_cvt_pk_bf16_f32 v21, v33, v34\n"
  "v_fma_f32 v25, v28, v32, v24\n"
  "v_fma_f32 v26, v29, v32, v24\n"
  "v_cvt_pk_bf16_f32 v22, v25, v26\n"
  "v_fma_f32 v33, v30, v32, v24\n"
  "v_fma_f32 v34, v31, v32, v24\n"
  "v_cvt_pk_bf16_f32 v23, v33, v34\n"
  "ds_write_b128 v251, v[20:23] offset:12288\n"
  "v_fma_f32 v25, v140, v32, v24\n"
  "v_fma_f32 v26, v141, v32, v24\n"
  "v_cvt_pk_bf16_f32 v254, v25, v26\n"
  "v_fma_f32 v33, v142, v32, v24\n"
  "v_fma_f32 v34, v143, v32, v24\n"
  "v_cvt_pk_bf16_f32 v255, v33, v34\n"
  "ds_write_b64 v247, v[254:255] offset:12288\n"
  "v_cvt_pk_bf16_f32 v177, v178, v179\n"
  "ds_write_b64 v244, v[176:177] offset:18688\n"
  "v_mul_f32 v61, v61, v124\n"
  "v_cvt_pk_bf16_f32 v122, v60, v61\n"
  "v_mul_f32 v62, v62, v124\n"
  "v_mul_f32 v63, v63, v124\n"
  "v_cvt_pk_bf16_f32 v123, v62, v63\n"
  "v_mul_f32 v112, v112, v124\n"
  "v_mul_f32 v113, v113, v124\n"
  "v_cvt_pk_bf16_f32 v112, v112, v113\n"
  "v_mul_f32 v114, v114, v124\n"
  "v_mul_f32 v115, v115, v124\n"
  "v_cvt_pk_bf16_f32 v113, v114, v115\n"
  "s_mov_b64 exec, s[66:67]\n"
  "ds_write_b128 v251, v[120:123] offset:33792\n"
  "ds_write_b64 v247, v[112:113] offset:33792\n"
  "s_mov_b64 exec, -1\n"
  "s_waitcnt lgkmcnt(3)\n"
  "s_barrier\n"
  "ds_read_b128 v[20:23], v252 offset:27648\n"
  "ds_read_b128 v[24:27], v252 offset:28672\n"
  "ds_read_b128 v[32:35], v252 offset:29696\n"
  "v_add_f32 v48, v48, v49\n"
  "v_add_f32 v50, v50, v51\n"
  "v_add_f32 v48, v48, v50\n"
  "v_add_f32 v104, v104, v105\n"
  "v_add_f32 v106, v106, v107\n"
  "v_add_f32 v104, v104, v106\n"
  "v_mul_f32 v48, 0xbbaaaaab, v48\n"
  "v_mul_f32 v104, 0x3baaaaab, v104\n"
  "v_fma_f32 v104, -v48, v48, v104\n"
  "v_add_f32_e64 v104, abs(v104), s43\n"
  "v_rsq_f32 v104, v104\n"
  "s_nop 0\n"
  "v_mul_f32 v48, v48, v104\n"
  "v_fma_f32 v49, v164, v104, v48\n"
  "v_fma_f32 v50, v165, v104, v48\n"
  "v_cvt_pk_bf16_f32 v60, v49, v50\n"
  "s_waitcnt lgkmcnt(2)\n"
  "v_mfma_f32_16x16x32_bf16 v[148:151], a[216:219], v[20:23], v[148:151]\n"
  "ds_read_b128 v[108:111], v253 offset:1024\n"
  "ds_read_b128 v[112:115], v253 offset:1280\n"
  "v_mfma_f32_16x16x32_bf16 v[212:215], a[232:235], v[20:23], v[212:215]\n"
  "s_waitcnt lgkmcnt(3)\n"
  "v_fma_f32 v105, v166, v104, v48\n"
  "v_fma_f32 v106, v167, v104, v48\n"
  "v_mfma_f32_16x16x32_bf16 v[56:59], a[244:247], v[20:23], v[56:59]\n"
  "s_waitcnt lgkmcnt(2)\n"
  "ds_read_b128 v[120:123], v252 offset:30720\n"
  "v_cvt_pk_bf16_f32 v61, v105, v106\n"
  "v_mfma_f32_16x16x32_bf16 v[148:151], a[220:223], v[24:27], v[148:151]\n"
  "v_fma_f32 v49, v132, v104, v48\n"
  "v_fma_f32 v50, v133, v104, v48\n"
  "v_mfma_f32_16x16x32_bf16 v[212:215], a[236:239], v[24:27], v[212:215]\n"
  "v_cvt_pk_bf16_f32 v62, v49, v50\n"
  "v_fma_f32 v105, v134, v104, v48\n"
  "v_mfma_f32_16x16x32_bf16 v[56:59], a[248:251], v[24:27], v[56:59]\n"
  "s_waitcnt lgkmcnt(2)\n"
  "ds_read_b128 v[20:23], v252 offset:31744\n"
  "v_fma_f32 v106, v135, v104, v48\n"
  "v_mfma_f32_16x16x32_bf16 v[148:151], a[224:227], v[32:35], v[148:151]\n"
  "s_waitcnt lgkmcnt(2)\n"
  "v_cvt_pk_bf16_f32 v63, v105, v106\n"
  "v_fma_f32 v49, v184, v104, v48\n"
  "v_mfma_f32_16x16x32_bf16 v[212:215], a[240:243], v[32:35], v[212:215]\n"
  "s_waitcnt lgkmcnt(1)\n"
  "ds_write_b128 v251, v[60:63] offset:18432\n"
  "v_mfma_f32_16x16x32_bf16 v[56:59], a[252:255], v[32:35], v[56:59]\n"
  "v_fma_f32 v50, v185, v104, v48\n"
  "v_cvt_pk_bf16_f32 v254, v49, v50\n"
  "v_mfma_f32_16x16x32_bf16 v[148:151], a[228:231], v[120:123], v[148:151]\n"
  "ds_read_b128 v[24:27], v252 offset:32768\n"
  "v_fma_f32 v105, v186, v104, v48\n"
  "v_mfma_f32_16x16x32_bf16 v[212:215], v[4:7], v[120:123], v[212:215]\n"
  "s_waitcnt lgkmcnt(2)\n"
  "v_fma_f32 v106, v187, v104, v48\n"
  "v_cvt_pk_bf16_f32 v255, v105, v106\n"
  "v_mfma_f32_16x16x32_bf16 v[56:59], v[76:79], v[120:123], v[56:59]\n"
  "ds_read_b128 v[32:35], v252 offset:12288\n"
  "ds_write_b64 v247, v[254:255] offset:18432\n"
  "v_mfma_f32_16x16x32_bf16 v[148:151], v[80:83], v[20:23], v[148:151]\n"
  "v_add_f32 v108, v108, v109\n"
  "v_add_f32 v110, v110, v111\n"
  "v_mfma_f32_16x16x32_bf16 v[212:215], v[84:87], v[20:23], v[212:215]\n"
  "v_add_f32 v108, v108, v110\n"
  "v_add_f32 v112, v112, v113\n"
  "v_mfma_f32_16x16x32_bf16 v[56:59], v[88:91], v[20:23], v[56:59]\n"
  "s_waitcnt lgkmcnt(2)\n"
  "v_add_f32 v114, v114, v115\n"
  "v_add_f32 v112, v112, v114\n"
  "v_mfma_f32_16x16x32_bf16 v[148:151], v[92:95], v[24:27], v[148:151]\n"
  "v_mul_f32 v108, 0xbbaaaaab, v108\n"
  "v_mul_f32 v112, 0x3baaaaab, v112\n"
  "v_mfma_f32_16x16x32_bf16 v[212:215], v[96:99], v[24:27], v[212:215]\n"
  "s_waitcnt lgkmcnt(1)\n"
  "v_fma_f32 v112, -v108, v108, v112\n"
  "v_add_f32_e64 v112, abs(v112), s43\n"
  "v_mfma_f32_16x16x32_bf16 v[56:59], v[100:103], v[24:27], v[56:59]\n"
  "ds_read_b128 v[20:23], v252 offset:13312\n"
  "v_cvt_pk_bf16_f32 v148, v148, v149\n"
  "v_mfma_f32_16x16x32_bf16 v[24:27], a[72:75], v[32:35], 0\n"
  "v_cvt_pk_bf16_f32 v149, v150, v151\n"
  "v_cvt_pk_bf16_f32 v212, v212, v213\n"
  "v_mfma_f32_16x16x32_bf16 v[48:51], a[96:99], v[32:35], 0\n"
  "ds_write_b64 v244, v[148:149] offset:0\n"
  "v_mfma_f32_16x16x32_bf16 v[60:63], a[120:123], v[32:35], 0\n"
  "v_cvt_pk_bf16_f32 v213, v214, v215\n"
  "v_cvt_pk_bf16_f32 v56, v56, v57\n"
  "v_mfma_f32_16x16x32_bf16 v[104:107], a[0:3], v[32:35], 0\n"
  "ds_write_b64 v244, v[212:213] offset:128\n"
  "v_mfma_f32_16x16x32_bf16 v[120:123], a[24:27], v[32:35], 0\n"
  "s_waitcnt lgkmcnt(2)\n"
  "v_cvt_pk_bf16_f32 v57, v58, v59\n"
  "v_mfma_f32_16x16x32_bf16 v[124:127], a[48:51], v[32:35], 0\n"
  "ds_write_b64 v244, v[56:57] offset:256\n"
  "v_mfma_f32_16x16x32_bf16 v[24:27], a[76:79], v[20:23], v[24:27]\n"
  "v_rsq_f32 v112, v112\n"
  "v_mfma_f32_16x16x32_bf16 v[48:51], a[100:103], v[20:23], v[48:51]\n"
  "ds_read_b128 v[32:35], v252 offset:14336\n"
  "v_mul_f32 v108, v108, v112\n"
  "v_mfma_f32_16x16x32_bf16 v[60:63], a[124:127], v[20:23], v[60:63]\n"
  "v_fma_f32 v109, v116, v112, v108\n"
  "v_fma_f32 v110, v117, v112, v108\n"
  "v_mfma_f32_16x16x32_bf16 v[104:107], a[4:7], v[20:23], v[104:107]\n"
  "v_cvt_pk_bf16_f32 v56, v109, v110\n"
  "v_fma_f32 v113, v118, v112, v108\n"
  "v_mfma_f32_16x16x32_bf16 v[120:123], a[28:31], v[20:23], v[120:123]\n"
  "v_fma_f32 v114, v119, v112, v108\n"
  "v_cvt_pk_bf16_f32 v57, v113, v114\n"
  "v_mfma_f32_16x16x32_bf16 v[124:127], a[52:55], v[20:23], v[124:127]\n"
  "ds_read_b128 v[136:139], v252 offset:15360\n"
  "v_fma_f32 v109, v152, v112, v108\n"
  "v_fma_f32 v110, v153, v112, v108\n"
  "v_cvt_pk_bf16_f32 v58, v109, v110\n"
  "s_waitcnt lgkmcnt(1)\n"
  "v_mfma_f32_16x16x32_bf16 v[24:27], a[80:83], v[32:35], v[24:27]\n"
  "v_fma_f32 v113, v154, v112, v108\n"
  "v_fma_f32 v114, v155, v112, v108\n"
  "v_mfma_f32_16x16x32_bf16 v[48:51], a[104:107], v[32:35], v[48:51]\n"
  "v_cvt_pk_bf16_f32 v59, v113, v114\n"
  "v_fma_f32 v109, v68, v112, v108\n"
  "v_mfma_f32_16x16x32_bf16 v[60:63], a[128:131], v[32:35], v[60:63]\n"
  "ds_write_b128 v251, v[56:59] offset:0\n"
  "v_mfma_f32_16x16x32_bf16 v[104:107], a[8:11], v[32:35], v[104:107]\n"
  "s_waitcnt lgkmcnt(1)\n"
  "v_fma_f32 v110, v69, v112, v108\n"
  "v_cvt_pk_bf16_f32 v254, v109, v110\n"
  "v_mfma_f32_16x16x32_bf16 v[120:123], a[32:35], v[32:35], v[120:123]\n"
  "v_fma_f32 v113, v70, v112, v108\n"
  "v_fma_f32 v114, v71, v112, v108\n"
  "v_mfma_f32_16x16x32_bf16 v[124:127], a[56:59], v[32:35], v[124:127]\n"
  "ds_read_b128 v[20:23], v252 offset:16384\n"
  "v_cvt_pk_bf16_f32 v255, v113, v114\n"
  "v_mfma_f32_16x16x32_bf16 v[24:27], a[84:87], v[136:139], v[24:27]\n"
  "ds_write_b64 v247, v[254:255] offset:0\n"
  "v_mfma_f32_16x16x32_bf16 v[48:51], a[108:111], v[136:139], v[48:51]\n"
  "v_mfma_f32_16x16x32_bf16 v[60:63], a[132:135], v[136:139], v[60:63]\n"
  "v_mfma_f32_16x16x32_bf16 v[104:107], a[12:15], v[136:139], v[104:107]\n"
  "v_mfma_f32_16x16x32_bf16 v[120:123], a[36:39], v[136:139], v[120:123]\n"
  "s_waitcnt lgkmcnt(1)\n"
  "v_mfma_f32_16x16x32_bf16 v[124:127], a[60:63], v[136:139], v[124:127]\n"
  "ds_read_b128 v[32:35], v252 offset:17408\n"
  "v_mfma_f32_16x16x32_bf16 v[24:27], a[88:91], v[20:23], v[24:27]\n"
  "s_waitcnt lgkmcnt(1)\n"
  "s_barrier\n"
  "v_mfma_f32_16x16x32_bf16 v[48:51], a[112:115], v[20:23], v[48:51]\n"
  "v_mfma_f32_16x16x32_bf16 v[60:63], a[136:139], v[20:23], v[60:63]\n"
  "v_mfma_f32_16x16x32_bf16 v[104:107], a[16:19], v[20:23], v[104:107]\n"
  "v_mfma_f32_16x16x32_bf16 v[120:123], a[40:43], v[20:23], v[120:123]\n"
  "s_waitcnt lgkmcnt(0)\n"
  "v_mfma_f32_16x16x32_bf16 v[124:127], a[64:67], v[20:23], v[124:127]\n"
  "v_mfma_f32_16x16x32_bf16 v[24:27], a[92:95], v[32:35], v[24:27]\n"
  "ds_read_b128 v[56:59], v252 offset:18432\n"
  "s_nop 0\n"
  "v_mfma_f32_16x16x32_bf16 v[48:51], a[116:119], v[32:35], v[48:51]\n"
  "s_nop 0\n"
  "s_nop 0\n"
  "v_mfma_f32_16x16x32_bf16 v[60:63], a[140:143], v[32:35], v[60:63]\n"
  "s_nop 0\n"
  "s_nop 0\n"
  "v_mfma_f32_16x16x32_bf16 v[104:107], a[20:23], v[32:35], v[104:107]\n"
  "ds_read_b128 v[20:23], v252 offset:19456\n"
  "v_cvt_pk_bf16_f32 v24, v24, v25\n"
  "v_mfma_f32_16x16x32_bf16 v[120:123], a[44:47], v[32:35], v[120:123]\n"
  "v_cvt_pk_bf16_f32 v25, v26, v27\n"
  "v_cvt_pk_bf16_f32 v26, v48, v49\n"
  "v_mfma_f32_16x16x32_bf16 v[124:127], a[68:71], v[32:35], v[124:127]\n"
  "s_waitcnt lgkmcnt(1)\n"
  "ds_read_b128 v[108:111], v252 offset:20480\n"
  "v_cvt_pk_bf16_f32 v27, v50, v51\n"
  "v_mfma_f32_16x16x32_bf16 v[32:35], a[72:75], v[56:59], 0\n"
  "v_cvt_pk_bf16_f32 v254, v60, v61\n"
  "v_cvt_pk_bf16_f32 v255, v62, v63\n"
  "v_mfma_f32_16x16x32_bf16 v[48:51], a[96:99], v[56:59], 0\n"
  "v_cvt_pk_bf16_f32 v104, v104, v105\n"
  "v_cvt_pk_bf16_f32 v105, v106, v107\n"
  "v_mfma_f32_16x16x32_bf16 v[60:63], a[120:123], v[56:59], 0\n"
  "s_waitcnt lgkmcnt(1)\n"
  "ds_read_b128 v[112:115], v252 offset:21504\n"
  "ds_read_u16 v253, v243 offset:24\n"
  "v_mfma_f32_16x16x32_bf16 v[32:35], a[76:79], v[20:23], v[32:35]\n"
  "ds_read_b128 v[56:59], v245 offset:12288\n"
  "ds_read_b128 v[136:139], v245 offset:12416\n"
  "v_mfma_f32_16x16x32_bf16 v[48:51], a[100:103], v[20:23], v[48:51]\n"
  "s_waitcnt lgkmcnt(4)\n"
  "ds_read_b128 v[144:147], v245 offset:12544\n"
  "v_cvt_pk_bf16_f32 v106, v120, v121\n"
  "v_mfma_f32_16x16x32_bf16 v[60:63], a[124:127], v[20:23], v[60:63]\n"
  "ds_read_b128 v[148:151], v252 offset:22528\n"
  "v_cvt_pk_bf16_f32 v107, v122, v123\n"
  "v_mfma_f32_16x16x32_bf16 v[32:35], a[80:83], v[108:111], v[32:35]\n"
  "v_cvt_pk_bf16_f32 v248, v124, v125\n"
  "v_cvt_pk_bf16_f32 v249, v126, v127\n"
  "v_mfma_f32_16x16x32_bf16 v[48:51], a[104:107], v[108:111], v[48:51]\n"
  "s_waitcnt lgkmcnt(5)\n"
  "v_mfma_f32_16x16x32_bf16 v[60:63], a[128:131], v[108:111], v[60:63]\n"
  "s_waitcnt lgkmcnt(4)\n"
  "ds_read_b128 v[20:23], v252 offset:23552\n"
  "v_mad_u32_u24 v253, v253, s42, v242\n"
  "v_mfma_f32_16x16x32_bf16 v[32:35], a[84:87], v[112:115], v[32:35]\n"
  "s_waitcnt lgkmcnt(4)\n"
  "buffer_store_dwordx4 v[56:59], v253, s[24:27], s41 offen offset:0 sc1\n"
  "v_mfma_f32_16x16x32_bf16 v[48:51], a[108:111], v[112:115], v[48:51]\n"
  "s_waitcnt lgkmcnt(1)\n"
  "buffer_store_dwordx4 v[136:139], v253, s[24:27], s41 offen offset:128 sc1\n"
  "v_mfma_f32_16x16x32_bf16 v[60:63], a[132:135], v[112:115], v[60:63]\n"
  "s_waitcnt lgkmcnt(0)\n"
  "ds_read_b128 v[56:59], v252 offset:0\n"
  "buffer_store_dwordx4 v[144:147], v253, s[24:27], s41 offen offset:256 sc1\n"
  "v_mfma_f32_16x16x32_bf16 v[32:35], a[88:91], v[148:151], v[32:35]\n"
  "v_mfma_f32_16x16x32_bf16 v[48:51], a[112:115], v[148:151], v[48:51]\n"
  "v_mfma_f32_16x16x32_bf16 v[60:63], a[136:139], v[148:151], v[60:63]\n"
  "s_waitcnt lgkmcnt(0)\n"
  "ds_read_b128 v[108:111], v252 offset:1024\n"
  "v_mfma_f32_16x16x32_bf16 v[32:35], a[92:95], v[20:23], v[32:35]\n"
  "s_nop 0\n"
  "s_nop 0\n"
  "v_mfma_f32_16x16x32_bf16 v[48:51], a[116:119], v[20:23], v[48:51]\n"
  "s_nop 0\n"
  "s_nop 0\n"
  "v_mfma_f32_16x16x32_bf16 v[60:63], a[140:143], v[20:23], v[60:63]\n"
  "ds_read_b128 v[112:115], v252 offset:2048\n"
  "s_nop 0\n"
  "v_mfma_f32_16x16x32_bf16 v[20:23], a[72:75], v[56:59], 0\n"
  "v_cvt_pk_bf16_f32 v32, v32, v33\n"
  "v_cvt_pk_bf16_f32 v33, v34, v35\n"
  "v_mfma_f32_16x16x32_bf16 v[120:123], a[96:99], v[56:59], 0\n"
  "s_waitcnt lgkmcnt(1)\n"
  "v_cvt_pk_bf16_f32 v34, v48, v49\n"
  "v_cvt_pk_bf16_f32 v35, v50, v51\n"
  "v_mfma_f32_16x16x32_bf16 v[124:127], a[120:123], v[56:59], 0\n"
  "ds_read_b128 v[136:139], v252 offset:3072\n"
  "v_cvt_pk_bf16_f32 v240, v60, v61\n"
  "v_mfma_f32_16x16x32_bf16 v[20:23], a[76:79], v[108:111], v[20:23]\n"
  "v_cvt_pk_bf16_f32 v241, v62, v63\n"
  "v_mfma_f32_16x16x32_bf16 v[120:123], a[100:103], v[108:111], v[120:123]\n"
  "s_waitcnt lgkmcnt(1)\n"
  "v_mfma_f32_16x16x32_bf16 v[124:127], a[124:127], v[108:111], v[124:127]\n"
  "ds_read_b128 v[48:51], v252 offset:4096\n"
  "v_mfma_f32_16x16x32_bf16 v[20:23], a[80:83], v[112:115], v[20:23]\n"
  "v_mfma_f32_16x16x32_bf16 v[120:123], a[104:107], v[112:115], v[120:123]\n"
  "s_waitcnt lgkmcnt(1)\n"
  "v_mfma_f32_16x16x32_bf16 v[124:127], a[128:131], v[112:115], v[124:127]\n"
  "ds_read_b128 v[56:59], v252 offset:5120\n"
  "v_mfma_f32_16x16x32_bf16 v[20:23], a[84:87], v[136:139], v[20:23]\n"
  "v_mfma_f32_16x16x32_bf16 v[120:123], a[108:111], v[136:139], v[120:123]\n"
  "s_waitcnt lgkmcnt(1)\n"
  "v_mfma_f32_16x16x32_bf16 v[124:127], a[132:135], v[136:139], v[124:127]\n"
  "v_mfma_f32_16x16x32_bf16 v[20:23], a[88:91], v[48:51], v[20:23]\n"
  "v_mfma_f32_16x16x32_bf16 v[120:123], a[112:115], v[48:51], v[120:123]\n"
  "s_waitcnt lgkmcnt(0)\n"
  "v_mfma_f32_16x16x32_bf16 v[124:127], a[136:139], v[48:51], v[124:127]\n"
  "v_mfma_f32_16x16x32_bf16 v[20:23], a[92:95], v[56:59], v[20:23]\n"
  "s_nop 0\n"
  "s_nop 0\n"
  "v_mfma_f32_16x16x32_bf16 v[120:123], a[116:119], v[56:59], v[120:123]\n"
  "s_nop 0\n"
  "s_nop 0\n"
  "v_mfma_f32_16x16x32_bf16 v[124:127], a[140:143], v[56:59], v[124:127]\n"
  "s_nop 0\n"
  "s_nop 0\n"
  "v_mfma_f32_16x16x32_bf16 v[48:51], v[64:67], v[128:131], v[10:13]\n"
  "v_cvt_pk_bf16_f32 v20, v20, v21\n"
  "v_cvt_pk_bf16_f32 v21, v22, v23\n"
  "v_mfma_f32_16x16x32_bf16 v[56:59], v[24:27], v[128:131], 0\n"
  "v_cvt_pk_bf16_f32 v22, v120, v121\n"
  "v_cvt_pk_bf16_f32 v23, v122, v123\n"
  "v_mfma_f32_16x16x32_bf16 v[60:63], v[32:35], v[128:131], 0\n"
  "v_cvt_pk_bf16_f32 v238, v124, v125\n"
  "v_cvt_pk_bf16_f32 v239, v126, v127\n"
  "v_mfma_f32_16x16x32_bf16 v[108:111], v[20:23], v[128:131], v[8:11]\n"
  "s_nop 0\n"
  "s_nop 0\n"
  "v_mfma_f32_16x16x16_bf16 v[48:51], v[234:235], v[14:15], v[48:51]\n"
  "s_nop 0\n"
  "s_nop 0\n"
  "v_mfma_f32_16x16x16_bf16 v[56:59], v[254:255], v[14:15], v[56:59]\n"
  "v_mfma_f32_16x16x16_bf16 v[60:63], v[240:241], v[14:15], v[60:63]\n"
  "v_mfma_f32_16x16x16_bf16 v[108:111], v[238:239], v[14:15], v[108:111]\n"
  "s_nop 0\n"
  "s_nop 0\n"
  "v_mfma_f32_16x16x32_bf16 v[112:115], v[64:67], v[104:107], v[10:13]\n"
  "s_nop 0\n"
  "s_nop 0\n"
  "v_mfma_f32_16x16x32_bf16 v[120:123], v[24:27], v[104:107], 0\n"
  "ds_read_b128 v[124:127], v252 offset:18432\n"
  "s_nop 0\n"
  "v_mfma_f32_16x16x32_bf16 v[128:131], v[32:35], v[104:107], 0\n"
  "v_max3_f32 v253, v108, v48, v49\n"
  "v_max3_f32 v253, v253, v50, v51\n"
  "v_mfma_f32_16x16x32_bf16 v[136:139], v[20:23], v[104:107], v[8:11]\n"
  "v_max3_f32 v253, v253, v56, v57\n"
  "v_max3_f32 v253, v253, v58, v59\n"
  "v_mfma_f32_16x16x16_bf16 v[112:115], v[234:235], v[248:249], v[112:115]\n"
  "ds_read_b128 v[104:107], v252 offset:19456\n"
  "v_max3_f32 v253, v253, v60, v61\n"
  "v_mfma_f32_16x16x16_bf16 v[120:123], v[254:255], v[248:249], v[120:123]\n"
  "v_max3_f32 v253, v253, v62, v63\n"
  "v_mfma_f32_16x16x16_bf16 v[128:131], v[240:241], v[248:249], v[128:131]\n"
  "s_waitcnt lgkmcnt(1)\n"
  "v_mfma_f32_16x16x16_bf16 v[136:139], v[238:239], v[248:249], v[136:139]\n"
  "ds_read_b128 v[144:147], v252 offset:20480\n"
  "s_nop 0\n"
  "v_mfma_f32_16x16x32_bf16 v[148:151], a[0:3], v[124:127], 0\n"
  "s_nop 0\n"
  "s_nop 0\n"
  "v_mfma_f32_16x16x32_bf16 v[156:159], a[24:27], v[124:127], 0\n"
  "s_waitcnt lgkmcnt(1)\n"
  "s_nop 0\n"
  "v_max3_f32 v250, v136, v112, v113\n"
  "v_mfma_f32_16x16x32_bf16 v[160:163], a[48:51], v[124:127], 0\n"
  "ds_read_b128 v[168:171], v252 offset:21504\n"
  "v_max3_f32 v250, v250, v114, v115\n"
  "v_mfma_f32_16x16x32_bf16 v[148:151], a[4:7], v[104:107], v[148:151]\n"
  "v_max3_f32 v250, v250, v120, v121\n"
  "v_max3_f32 v250, v250, v122, v123\n"
  "v_mfma_f32_16x16x32_bf16 v[156:159], a[28:31], v[104:107], v[156:159]\n"
  "s_waitcnt lgkmcnt(1)\n"
  "v_max3_f32 v250, v250, v128, v129\n"
  "v_max3_f32 v250, v250, v130, v131\n"
  "v_mfma_f32_16x16x32_bf16 v[160:163], a[52:55], v[104:107], v[160:163]\n"
  "ds_read_b128 v[124:127], v252 offset:22528\n"
  "v_mfma_f32_16x16x32_bf16 v[148:151], a[8:11], v[144:147], v[148:151]\n"
  "v_permlane16_swap_b32 v253, v250\n"
  "v_mfma_f32_16x16x32_bf16 v[156:159], a[32:35], v[144:147], v[156:159]\n"
  "s_waitcnt lgkmcnt(1)\n"
  "v_max_f32 v253, v253, v250\n"
  "v_mov_b32 v250, v253\n"
  "v_mfma_f32_16x16x32_bf16 v[160:163], a[56:59], v[144:147], v[160:163]\n"
  "ds_read_b128 v[104:107], v252 offset:23552\n"
  "v_mfma_f32_16x16x32_bf16 v[148:151], a[12:15], v[168:171], v[148:151]\n"
  "v_permlane32_swap_b32 v253, v250\n"
  "v_mfma_f32_16x16x32_bf16 v[156:159], a[36:39], v[168:171], v[156:159]\n"
  "s_waitcnt lgkmcnt(1)\n"
  "v_max_f32 v253, v253, v250\n"
  "v_mul_f32 v253, 0xbe553b95, v253\n"
  "v_mfma_f32_16x16x32_bf16 v[160:163], a[60:63], v[168:171], v[160:163]\n"
  "ds_read_b128 v[144:147], v252 offset:0\n"
  "v_mov_b32 v250, v253\n"
  "v_mfma_f32_16x16x32_bf16 v[148:151], a[16:19], v[124:127], v[148:151]\n"
  "s_nop 0\n"
  "v_mfma_f32_16x16x32_bf16 v[156:159], a[40:43], v[124:127], v[156:159]\n"
  "s_waitcnt lgkmcnt(1)\n"
  "v_permlane16_swap_b32 v253, v250\n"
  "v_mfma_f32_16x16x32_bf16 v[160:163], a[64:67], v[124:127], v[160:163]\n"
  "ds_read_b128 v[168:171], v252 offset:1024\n"
  "v_fmamk_f32 v56, v56, 0x3e553b95, v253\n"
  "v_mfma_f32_16x16x32_bf16 v[148:151], a[20:23], v[104:107], v[148:151]\n"
  "v_exp_f32 v56, v56\n"
  "v_mfma_f32_16x16x32_bf16 v[156:159], a[44:47], v[104:107], v[156:159]\n"
  "s_waitcnt lgkmcnt(1)\n"
  "v_fmamk_f32 v57, v57, 0x3e553b95, v253\n"
  "v_fmamk_f32 v58, v58, 0x3e553b95, v253\n"
  "v_mfma_f32_16x16x32_bf16 v[160:163], a[68:71], v[104:107], v[160:163]\n"
  "ds_read_b128 v[124:127], v252 offset:2048\n"
  "v_fmamk_f32 v59, v59, 0x3e553b95, v253\n"
  "v_mfma_f32_16x16x32_bf16 v[104:107], a[0:3], v[144:147], 0\n"
  "v_cvt_pk_bf16_f32 v148, v148, v149\n"
  "v_cvt_pk_bf16_f32 v149, v150, v151\n"
  "v_mfma_f32_16x16x32_bf16 v[172:175], a[24:27], v[144:147], 0\n"
  "s_waitcnt lgkmcnt(1)\n"
  "v_cvt_pk_bf16_f32 v150, v156, v157\n"
  "v_cvt_pk_bf16_f32 v151, v158, v159\n"
  "v_mfma_f32_16x16x32_bf16 v[176:179], a[48:51], v[144:147], 0\n"
  "ds_read_b128 v[180:183], v252 offset:3072\n"
  "v_cvt_pk_bf16_f32 v248, v160, v161\n"
  "v_mfma_f32_16x16x32_bf16 v[104:107], a[4:7], v[168:171], v[104:107]\n"
  "v_cvt_pk_bf16_f32 v249, v162, v163\n"
  "v_fmamk_f32 v48, v48, 0x3e553b95, v253\n"
  "v_mfma_f32_16x16x32_bf16 v[172:175], a[28:31], v[168:171], v[172:175]\n"
  "s_waitcnt lgkmcnt(1)\n"
  "v_exp_f32 v57, v57\n"
  "v_mfma_f32_16x16x32_bf16 v[176:179], a[52:55], v[168:171], v[176:179]\n"
  "ds_read_b128 v[144:147], v252 offset:4096\n"
  "v_cvt_pk_bf16_f32 v56, v56, v57\n"
  "v_mfma_f32_16x16x32_bf16 v[104:107], a[8:11], v[124:127], v[104:107]\n"
  "v_exp_f32 v58, v58\n"
  "v_mfma_f32_16x16x32_bf16 v[172:175], a[32:35], v[124:127], v[172:175]\n"
  "s_waitcnt lgkmcnt(1)\n"
  "v_exp_f32 v59, v59\n"
  "v_mfma_f32_16x16x32_bf16 v[176:179], a[56:59], v[124:127], v[176:179]\n"
  "ds_read_b128 v[156:159], v252 offset:5120\n"
  "v_cvt_pk_bf16_f32 v57, v58, v59\n"
  "v_mfma_f32_16x16x32_bf16 v[104:107], a[12:15], v[180:183], v[104:107]\n"
  "v_exp_f32 v48, v48\n"
  "v_mfma_f32_16x16x32_bf16 v[172:175], a[36:39], v[180:183], v[172:175]\n"
  "s_waitcnt lgkmcnt(1)\n"
  "v_fmamk_f32 v49, v49, 0x3e553b95, v253\n"
  "v_fmamk_f32 v50, v50, 0x3e553b95, v253\n"
  "v_mfma_f32_16x16x32_bf16 v[176:179], a[60:63], v[180:183], v[176:179]\n"
  "v_exp_f32 v49, v49\n"
  "v_mfma_f32_16x16x32_bf16 v[104:107], a[16:19], v[144:147], v[104:107]\n"
  "v_exp_f32 v50, v50\n"
  "v_mfma_f32_16x16x32_bf16 v[172:175], a[40:43], v[144:147], v[172:175]\n"
  "s_waitcnt lgkmcnt(0)\n"
  "v_cvt_pk_bf16_f32 v58, v48, v49\n"
  "v_fmamk_f32 v51, v51, 0x3e553b95, v253\n"
  "v_mfma_f32_16x16x32_bf16 v[176:179], a[64:67], v[144:147], v[176:179]\n"
  "v_exp_f32 v51, v51\n"
  "v_mfma_f32_16x16x32_bf16 v[104:107], a[20:23], v[156:159], v[104:107]\n"
  "v_cvt_pk_bf16_f32 v59, v50, v51\n"
  "v_fmamk_f32 v246, v108, 0x3e553b95, v253\n"
  "v_mfma_f32_16x16x32_bf16 v[172:175], a[44:47], v[156:159], v[172:175]\n"
  "v_exp_f32 v246, v246\n"
  "v_mfma_f32_16x16x32_bf16 v[176:179], a[68:71], v[156:159], v[176:179]\n"
  "v_fmamk_f32 v60, v60, 0x3e553b95, v253\n"
  "v_fmamk_f32 v61, v61, 0x3e553b95, v253\n"
  "v_mfma_f32_16x16x32_bf16 v[48:51], v[64:67], v[148:151], v[10:13]\n"
  "v_cvt_pk_bf16_f32 v104, v104, v105\n"
  "v_cvt_pk_bf16_f32 v105, v106, v107\n"
  "v_mfma_f32_16x16x32_bf16 v[108:111], v[24:27], v[148:151], 0\n"
  "v_cvt_pk_bf16_f32 v106, v172, v173\n"
  "v_cvt_pk_bf16_f32 v107, v174, v175\n"
  "v_mfma_f32_16x16x32_bf16 v[124:127], v[32:35], v[148:151], 0\n"
  "v_cvt_pk_bf16_f32 v236, v176, v177\n"
  "v_cvt_pk_bf16_f32 v237, v178, v179\n"
  "v_mfma_f32_16x16x32_bf16 v[144:147], v[20:23], v[148:151], v[8:11]\n"
  "v_exp_f32 v60, v60\n"
  "v_mfma_f32_16x16x16_bf16 v[48:51], v[234:235], v[248:249], v[48:51]\n"
  "v_exp_f32 v61, v61\n"
  "v_mfma_f32_16x16x16_bf16 v[108:111], v[254:255], v[248:249], v[108:111]\n"
  "v_cvt_pk_bf16_f32 v60, v60, v61\n"
  "v_fmamk_f32 v62, v62, 0x3e553b95, v253\n"
  "v_mfma_f32_16x16x16_bf16 v[124:127], v[240:241], v[248:249], v[124:127]\n"
  "v_exp_f32 v62, v62\n"
  "v_mfma_f32_16x16x16_bf16 v[144:147], v[238:239], v[248:249], v[144:147]\n"
  "v_fmamk_f32 v63, v63, 0x3e553b95, v253\n"
  "v_fmamk_f32 v120, v120, 0x3e553b95, v250\n"
  "v_mfma_f32_16x16x32_bf16 v[148:151], v[64:67], v[104:107], v[10:13]\n"
  "v_exp_f32 v63, v63\n"
  "v_mfma_f32_16x16x32_bf16 v[156:159], v[24:27], v[104:107], 0\n"
  "ds_read_b128 v[64:67], v252 offset:12288\n"
  "v_cvt_pk_bf16_f32 v61, v62, v63\n"
  "v_mfma_f32_16x16x32_bf16 v[24:27], v[32:35], v[104:107], 0\n"
  "v_exp_f32 v120, v120\n"
  "v_mfma_f32_16x16x32_bf16 v[160:163], v[20:23], v[104:107], v[8:11]\n"
  "v_cvt_pk_bf16_f32 v62, v246, 0\n"
  "v_mov_b32 v63, 0\n"
  "v_mfma_f32_16x16x16_bf16 v[148:151], v[234:235], v[236:237], v[148:151]\n"
  "ds_read_b128 v[8:11], v252 offset:13312\n"
  "v_fmamk_f32 v121, v121, 0x3e553b95, v250\n"
  "v_mfma_f32_16x16x16_bf16 v[156:159], v[254:255], v[236:237], v[156:159]\n"
  "v_exp_f32 v121, v121\n"
  "v_mfma_f32_16x16x16_bf16 v[24:27], v[240:241], v[236:237], v[24:27]\n"
  "s_waitcnt lgkmcnt(1)\n"
  "v_cvt_pk_bf16_f32 v120, v120, v121\n"
  "v_fmamk_f32 v122, v122, 0x3e553b95, v250\n"
  "v_mfma_f32_16x16x16_bf16 v[160:163], v[238:239], v[236:237], v[160:163]\n"
  "ds_read_b128 v[12:15], v252 offset:14336\n"
  "v_fmamk_f32 v123, v123, 0x3e553b95, v250\n"
  "v_mfma_f32_16x16x32_bf16 v[20:23], v[64:67], a[144:147], 0\n"
  "v_exp_f32 v122, v122\n"
  "v_mfma_f32_16x16x32_bf16 v[32:35], v[64:67], a[168:171], 0\n"
  "s_waitcnt lgkmcnt(1)\n"
  "v_exp_f32 v123, v123\n"
  "v_mfma_f32_16x16x32_bf16 v[104:107], v[64:67], a[192:195], 0\n"
  "ds_read_b128 v[168:171], v252 offset:15360\n"
  "v_cvt_pk_bf16_f32 v121, v122, v123\n"
  "v_mfma_f32_16x16x32_bf16 v[20:23], v[8:11], a[148:151], v[20:23]\n"
  "v_fmamk_f32 v112, v112, 0x3e553b95, v250\n"
  "v_fmamk_f32 v113, v113, 0x3e553b95, v250\n"
  "v_mfma_f32_16x16x32_bf16 v[32:35], v[8:11], a[172:175], v[32:35]\n"
  "s_waitcnt lgkmcnt(1)\n"
  "v_exp_f32 v112, v112\n"
  "v_mfma_f32_16x16x32_bf16 v[104:107], v[8:11], a[196:199], v[104:107]\n"
  "ds_read_b128 v[64:67], v252 offset:16384\n"
  "v_fmamk_f32 v114, v114, 0x3e553b95, v250\n"
  "v_mfma_f32_16x16x32_bf16 v[20:23], v[12:15], a[152:155], v[20:23]\n"
  "v_exp_f32 v113, v113\n"
  "v_mfma_f32_16x16x32_bf16 v[32:35], v[12:15], a[176:179], v[32:35]\n"
  "s_waitcnt lgkmcnt(1)\n"
  "v_exp_f32 v114, v114\n"
  "v_mfma_f32_16x16x32_bf16 v[104:107], v[12:15], a[200:203], v[104:107]\n"
  "ds_read_b128 v[8:11], v252 offset:17408\n"
  "v_cvt_pk_bf16_f32 v122, v112, v113\n"
  "v_mfma_f32_16x16x32_bf16 v[20:23], v[168:171], a[156:159], v[20:23]\n"
  "v_fmamk_f32 v115, v115, 0x3e553b95, v250\n"
  "v_fmamk_f32 v255, v136, 0x3e553b95, v250\n"
  "v_mfma_f32_16x16x32_bf16 v[32:35], v[168:171], a[180:183], v[32:35]\n"
  "s_waitcnt lgkmcnt(1)\n"
  "v_exp_f32 v115, v115\n"
  "v_mfma_f32_16x16x32_bf16 v[104:107], v[168:171], a[204:207], v[104:107]\n"
  "ds_read_b128 v[12:15], v252 offset:18432\n"
  "v_cvt_pk_bf16_f32 v123, v114, v115\n"
  "v_mfma_f32_16x16x32_bf16 v[20:23], v[64:67], a[160:163], v[20:23]\n"
  "v_exp_f32 v255, v255\n"
  "v_mfma_f32_16x16x32_bf16 v[32:35], v[64:67], a[184:187], v[32:35]\n"
  "s_waitcnt lgkmcnt(1)\n"
  "v_fmamk_f32 v128, v128, 0x3e553b95, v250\n"
  "v_fmamk_f32 v129, v129, 0x3e553b95, v250\n"
  "v_mfma_f32_16x16x32_bf16 v[104:107], v[64:67], a[208:211], v[104:107]\n"
  "ds_read_b128 v[112:115], v252 offset:19456\n"
  "v_fmamk_f32 v130, v130, 0x3e553b95, v250\n"
  "v_mfma_f32_16x16x32_bf16 v[20:23], v[8:11], a[164:167], v[20:23]\n"
  "v_exp_f32 v128, v128\n"
  "v_mfma_f32_16x16x32_bf16 v[32:35], v[8:11], a[188:191], v[32:35]\n"
  "s_waitcnt lgkmcnt(1)\n"
  "v_exp_f32 v129, v129\n"
  "v_mfma_f32_16x16x32_bf16 v[104:107], v[8:11], a[212:215], v[104:107]\n"
  "ds_read_b128 v[64:67], v252 offset:20480\n"
  "v_cvt_pk_bf16_f32 v128, v128, v129\n"
  "v_mfma_f32_16x16x32_bf16 v[8:11], v[12:15], a[144:147], 0\n"
  "v_cvt_pk_bf16_f32 v200, v20, v21\n"
  "v_cvt_pk_bf16_f32 v201, v22, v23\n"
  "v_mfma_f32_16x16x32_bf16 v[136:139], v[12:15], a[168:171], 0\n"
  "s_waitcnt lgkmcnt(1)\n"
  "v_cvt_pk_bf16_f32 v72, v32, v33\n"
  "v_cvt_pk_bf16_f32 v73, v34, v35\n"
  "v_mfma_f32_16x16x32_bf16 v[20:23], v[12:15], a[192:195], 0\n"
  "ds_read_b128 v[168:171], v252 offset:21504\n"
  "v_cvt_pk_bf16_f32 v188, v104, v105\n"
  "v_mfma_f32_16x16x32_bf16 v[8:11], v[112:115], a[148:151], v[8:11]\n"
  "v_cvt_pk_bf16_f32 v189, v106, v107\n"
  "v_fmamk_f32 v131, v131, 0x3e553b95, v250\n"
  "v_mfma_f32_16x16x32_bf16 v[136:139], v[112:115], a[172:175], v[136:139]\n"
  "s_waitcnt lgkmcnt(1)\n"
  "v_exp_f32 v130, v130\n"
  "v_mfma_f32_16x16x32_bf16 v[20:23], v[112:115], a[196:199], v[20:23]\n"
  "ds_read_b128 v[12:15], v252 offset:22528\n"
  "v_max3_f32 v254, v144, v48, v49\n"
  "v_mfma_f32_16x16x32_bf16 v[8:11], v[64:67], a[152:155], v[8:11]\n"
  "v_exp_f32 v131, v131\n"
  "v_mfma_f32_16x16x32_bf16 v[136:139], v[64:67], a[176:179], v[136:139]\n"
  "s_waitcnt lgkmcnt(1)\n"
  "v_cvt_pk_bf16_f32 v129, v130, v131\n"
  "v_cvt_pk_bf16_f32 v130, v255, 0\n"
  "v_mfma_f32_16x16x32_bf16 v[20:23], v[64:67], a[200:203], v[20:23]\n"
  "ds_read_b128 v[32:35], v252 offset:23552\n"
  "v_mov_b32 v131, 0\n"
  "v_mfma_f32_16x16x32_bf16 v[8:11], v[168:171], a[156:159], v[8:11]\n"
  "v_max3_f32 v254, v254, v50, v51\n"
  "v_max3_f32 v254, v254, v108, v109\n"
  "v_mfma_f32_16x16x32_bf16 v[136:139], v[168:171], a[180:183], v[136:139]\n"
  "s_waitcnt lgkmcnt(1)\n"
  "v_max3_f32 v254, v254, v110, v111\n"
  "v_max3_f32 v254, v254, v124, v125\n"
  "v_mfma_f32_16x16x32_bf16 v[20:23], v[168:171], a[204:207], v[20:23]\n"
  "ds_read_b128 v[64:67], v252 offset:0\n"
  "v_max3_f32 v254, v254, v126, v127\n"
  "v_mfma_f32_16x16x32_bf16 v[8:11], v[12:15], a[160:163], v[8:11]\n"
  "v_max3_f32 v255, v160, v148, v149\n"
  "v_max3_f32 v255, v255, v150, v151\n"
  "v_mfma_f32_16x16x32_bf16 v[136:139], v[12:15], a[184:187], v[136:139]\n"
  "s_waitcnt lgkmcnt(1)\n"
  "v_max3_f32 v255, v255, v156, v157\n"
  "v_max3_f32 v255, v255, v158, v159\n"
  "v_mfma_f32_16x16x32_bf16 v[20:23], v[12:15], a[208:211], v[20:23]\n"
  "ds_read_b128 v[104:107], v252 offset:1024\n"
  "v_max3_f32 v255, v255, v24, v25\n"
  "v_mfma_f32_16x16x32_bf16 v[8:11], v[32:35], a[164:167], v[8:11]\n"
  "v_max3_f32 v255, v255, v26, v27\n"
  "s_nop 0\n"
  "v_mfma_f32_16x16x32_bf16 v[136:139], v[32:35], a[188:191], v[136:139]\n"
  "s_waitcnt lgkmcnt(1)\n"
  "v_permlane16_swap_b32 v254, v255\n"
  "v_mfma_f32_16x16x32_bf16 v[20:23], v[32:35], a[212:215], v[20:23]\n"
  "ds_read_b128 v[12:15], v252 offset:2048\n"
  "v_max_f32 v254, v254, v255\n"
  "v_mfma_f32_16x16x32_bf16 v[32:35], v[64:67], a[144:147], 0\n"
  "v_cvt_pk_bf16_f32 v112, v8, v9\n"
  "v_cvt_pk_bf16_f32 v113, v10, v11\n"
  "v_mfma_f32_16x16x32_bf16 v[168:171], v[64:67], a[168:171], 0\n"
  "s_waitcnt lgkmcnt(1)\n"
  "v_cvt_pk_bf16_f32 v8, v136, v137\n"
  "v_cvt_pk_bf16_f32 v9, v138, v139\n"
  "v_mfma_f32_16x16x32_bf16 v[172:175], v[64:67], a[192:195], 0\n"
  "ds_read_b128 v[176:179], v252 offset:3072\n"
  "v_cvt_pk_bf16_f32 v136, v20, v21\n"
  "v_mfma_f32_16x16x32_bf16 v[32:35], v[104:107], a[148:151], v[32:35]\n"
  "v_cvt_pk_bf16_f32 v137, v22, v23\n"
  "v_mov_b32 v255, v254\n"
  "v_mfma_f32_16x16x32_bf16 v[168:171], v[104:107], a[172:175], v[168:171]\n"
  "s_waitcnt lgkmcnt(1)\n"
  "v_permlane32_swap_b32 v254, v255\n"
  "v_mfma_f32_16x16x32_bf16 v[172:175], v[104:107], a[196:199], v[172:175]\n"
  "ds_read_b128 v[20:23], v252 offset:4096\n"
  "v_max_f32 v254, v254, v255\n"
  "v_mfma_f32_16x16x32_bf16 v[32:35], v[12:15], a[152:155], v[32:35]\n"
  "v_mul_f32 v254, 0xbe553b95, v254\n"
  "v_mov_b32 v255, v254\n"
  "v_mfma_f32_16x16x32_bf16 v[168:171], v[12:15], a[176:179], v[168:171]\n"
  "s_waitcnt lgkmcnt(1)\n"
  "v_permlane16_swap_b32 v254, v255\n"
  "v_mfma_f32_16x16x32_bf16 v[172:175], v[12:15], a[200:203], v[172:175]\n"
  "ds_read_b128 v[64:67], v252 offset:5120\n"
  "v_fmamk_f32 v108, v108, 0x3e553b95, v254\n"
  "v_mfma_f32_16x16x32_bf16 v[32:35], v[176:179], a[156:159], v[32:35]\n"
  "v_exp_f32 v108, v108\n"
  "v_mfma_f32_16x16x32_bf16 v[168:171], v[176:179], a[180:183], v[168:171]\n"
  "s_waitcnt lgkmcnt(1)\n"
  "v_fmamk_f32 v109, v109, 0x3e553b95, v254\n"
  "v_fmamk_f32 v110, v110, 0x3e553b95, v254\n"
  "v_mfma_f32_16x16x32_bf16 v[172:175], v[176:179], a[204:207], v[172:175]\n"
  "v_exp_f32 v109, v109\n"
  "v_mfma_f32_16x16x32_bf16 v[32:35], v[20:23], a[160:163], v[32:35]\n"
  "v_exp_f32 v110, v110\n"
  "v_mfma_f32_16x16x32_bf16 v[168:171], v[20:23], a[184:187], v[168:171]\n"
  "s_waitcnt lgkmcnt(0)\n"
  "v_cvt_pk_bf16_f32 v108, v108, v109\n"
  "v_fmamk_f32 v111, v111, 0x3e553b95, v254\n"
  "v_mfma_f32_16x16x32_bf16 v[172:175], v[20:23], a[208:211], v[172:175]\n"
  "v_exp_f32 v111, v111\n"
  "v_mfma_f32_16x16x32_bf16 v[32:35], v[64:67], a[164:167], v[32:35]\n"
  "v_cvt_pk_bf16_f32 v109, v110, v111\n"
  "v_fmamk_f32 v48, v48, 0x3e553b95, v254\n"
  "v_mfma_f32_16x16x32_bf16 v[168:171], v[64:67], a[188:191], v[168:171]\n"
  "v_exp_f32 v48, v48\n"
  "v_mfma_f32_16x16x32_bf16 v[172:175], v[64:67], a[212:215], v[172:175]\n"
  "v_fmamk_f32 v49, v49, 0x3e553b95, v254\n"
  "v_fmamk_f32 v50, v50, 0x3e553b95, v254\n"
  "v_mfma_f32_16x16x32_bf16 v[12:15], v[16:19], v[56:59], 0\n"
  "v_cvt_pk_bf16_f32 v114, v32, v33\n"
  "v_cvt_pk_bf16_f32 v115, v34, v35\n"
  "v_mfma_f32_16x16x32_bf16 v[20:23], v[200:203], v[56:59], 0\n"
  "v_cvt_pk_bf16_f32 v10, v168, v169\n"
  "v_cvt_pk_bf16_f32 v11, v170, v171\n"
  "v_mfma_f32_16x16x32_bf16 v[32:35], v[72:75], v[56:59], 0\n"
  "v_cvt_pk_bf16_f32 v138, v172, v173\n"
  "v_cvt_pk_bf16_f32 v139, v174, v175\n"
  "v_mfma_f32_16x16x32_bf16 v[64:67], v[188:191], v[56:59], 0\n"
  "v_exp_f32 v49, v49\n"
  "v_mfma_f32_16x16x32_bf16 v[12:15], v[16:19], v[60:63], v[12:15]\n"
  "v_exp_f32 v50, v50\n"
  "v_mfma_f32_16x16x32_bf16 v[20:23], v[112:115], v[60:63], v[20:23]\n"
  "v_cvt_pk_bf16_f32 v110, v48, v49\n"
  "v_fmamk_f32 v51, v51, 0x3e553b95, v254\n"
  "v_mfma_f32_16x16x32_bf16 v[32:35], v[8:11], v[60:63], v[32:35]\n"
  "v_exp_f32 v51, v51\n"
  "v_mfma_f32_16x16x32_bf16 v[64:67], v[136:139], v[60:63], v[64:67]\n"
  "v_cvt_pk_bf16_f32 v111, v50, v51\n"
  "v_fmamk_f32 v253, v144, 0x3e553b95, v254\n"
  "v_mfma_f32_16x16x32_bf16 v[56:59], v[16:19], v[120:123], 0\n"
  "v_rcp_f32 v12, v12\n"
  "v_mfma_f32_16x16x32_bf16 v[48:51], v[200:203], v[120:123], 0\n"
  "v_mul_f32 v20, v20, v12\n"
  "v_mul_f32 v21, v21, v12\n"
  "v_mfma_f32_16x16x32_bf16 v[60:63], v[72:75], v[120:123], 0\n"
  "v_cvt_pk_bf16_f32 v20, v20, v21\n"
  "v_mul_f32 v22, v22, v12\n"
  "v_mfma_f32_16x16x32_bf16 v[104:107], v[188:191], v[120:123], 0\n"
  "v_mul_f32 v23, v23, v12\n"
  "v_cvt_pk_bf16_f32 v21, v22, v23\n"
  "v_mfma_f32_16x16x32_bf16 v[56:59], v[16:19], v[128:131], v[56:59]\n"
  "v_mul_f32 v32, v32, v12\n"
  "v_mul_f32 v33, v33, v12\n"
  "v_mfma_f32_16x16x32_bf16 v[48:51], v[112:115], v[128:131], v[48:51]\n"
  "v_cvt_pk_bf16_f32 v22, v32, v33\n"
  "v_mul_f32 v34, v34, v12\n"
  "v_mfma_f32_16x16x32_bf16 v[60:63], v[8:11], v[128:131], v[60:63]\n"
  "v_mul_f32 v35, v35, v12\n"
  "v_cvt_pk_bf16_f32 v23, v34, v35\n"
  "v_mfma_f32_16x16x32_bf16 v[104:107], v[136:139], v[128:131], v[104:107]\n"
  "v_mul_f32 v64, v64, v12\n"
  "v_mul_f32 v65, v65, v12\n"
  "v_cvt_pk_bf16_f32 v64, v64, v65\n"
  "v_mul_f32 v66, v66, v12\n"
  "v_mul_f32 v67, v67, v12\n"
  "v_cvt_pk_bf16_f32 v65, v66, v67\n"
  "s_mov_b64 exec, s[60:61]\n"
  "ds_write_b128 v251, v[20:23] offset:33792\n"
  "ds_write_b64 v247, v[64:65] offset:33792\n"
  "s_mov_b64 exec, -1\n"
  "v_rcp_f32 v56, v56\n"
  "v_exp_f32 v253, v253\n"
  "v_mul_f32 v48, v48, v56\n"
  "v_mul_f32 v49, v49, v56\n"
  "v_cvt_pk_bf16_f32 v48, v48, v49\n"
  "v_mul_f32 v50, v50, v56\n"
  "v_mul_f32 v51, v51, v56\n"
  "v_cvt_pk_bf16_f32 v49, v50, v51\n"
  "v_mul_f32 v60, v60, v56\n"
  "v_mul_f32 v61, v61, v56\n"
  "v_cvt_pk_bf16_f32 v50, v60, v61\n"
  "v_mul_f32 v62, v62, v56\n"
  "v_mul_f32 v63, v63, v56\n"
  "v_cvt_pk_bf16_f32 v51, v62, v63\n"
  "ds_write_b128 v251, v[48:51] offset:39936\n"
  "v_mul_f32 v104, v104, v56\n"
  "v_mul_f32 v105, v105, v56\n"
  "v_cvt_pk_bf16_f32 v104, v104, v105\n"
  "v_mul_f32 v106, v106, v56\n"
  "v_mul_f32 v107, v107, v56\n"
  "v_cvt_pk_bf16_f32 v105, v106, v107\n"
  "ds_write_b64 v247, v[104:105] offset:39936\n"
  "v_fmamk_f32 v124, v124, 0x3e553b95, v254\n"
  "v_exp_f32 v124, v124\n"
  "v_fmamk_f32 v125, v125, 0x3e553b95, v254\n"
  "v_exp_f32 v125, v125\n"
  "v_fmamk_f32 v126, v126, 0x3e553b95, v254\n"
  "v_cvt_pk_bf16_f32 v124, v124, v125\n"
  "v_exp_f32 v126, v126\n"
  "v_fmamk_f32 v127, v127, 0x3e553b95, v254\n"
  "v_exp_f32 v127, v127\n"
  "v_fmamk_f32 v156, v156, 0x3e553b95, v255\n"
  "v_cvt_pk_bf16_f32 v125, v126, v127\n"
  "v_cvt_pk_bf16_f32 v126, v253, 0\n"
  "v_mov_b32 v127, 0\n"
  "v_exp_f32 v156, v156\n"
  "v_fmamk_f32 v157, v157, 0x3e553b95, v255\n"
  "v_exp_f32 v157, v157\n"
  "s_waitcnt lgkmcnt(0)\n"
  "s_barrier\n"
  "v_mfma_f32_16x16x32_bf16 v[12:15], v[16:19], v[108:111], 0\n"
  "ds_read_b128 v[20:23], v252 offset:33792\n"
  "ds_read_b128 v[32:35], v252 offset:34816\n"
  "ds_read_b128 v[48:51], v252 offset:35840\n"
  "v_cvt_pk_bf16_f32 v156, v156, v157\n"
  "v_fmamk_f32 v158, v158, 0x3e553b95, v255\n"
  "v_exp_f32 v158, v158\n"
  "v_fmamk_f32 v159, v159, 0x3e553b95, v255\n"
  "v_exp_f32 v159, v159\n"
  "v_fmamk_f32 v148, v148, 0x3e553b95, v255\n"
  "v_cvt_pk_bf16_f32 v157, v158, v159\n"
  "v_exp_f32 v148, v148\n"
  "v_fmamk_f32 v149, v149, 0x3e553b95, v255\n"
  "v_exp_f32 v149, v149\n"
  "v_fmamk_f32 v150, v150, 0x3e553b95, v255\n"
  "v_exp_f32 v150, v150\n"
  "s_waitcnt lgkmcnt(2)\n"
  "v_mfma_f32_16x16x32_bf16 v[36:39], a[216:219], v[20:23], v[36:39]\n"
  "v_cvt_pk_bf16_f32 v158, v148, v149\n"
  "v_fmamk_f32 v151, v151, 0x3e553b95, v255\n"
  "v_mfma_f32_16x16x32_bf16 v[40:43], a[232:235], v[20:23], v[40:43]\n"
  "s_waitcnt lgkmcnt(1)\n"
  "v_exp_f32 v151, v151\n"
  "v_mfma_f32_16x16x32_bf16 v[44:47], a[244:247], v[20:23], v[44:47]\n"
  "s_waitcnt lgkmcnt(0)\n"
  "ds_read_b128 v[56:59], v252 offset:36864\n"
  "v_cvt_pk_bf16_f32 v159, v150, v151\n"
  "v_mfma_f32_16x16x32_bf16 v[36:39], a[220:223], v[32:35], v[36:39]\n"
  "v_fmamk_f32 v254, v160, 0x3e553b95, v255\n"
  "v_fmamk_f32 v24, v24, 0x3e553b95, v255\n"
  "v_mfma_f32_16x16x32_bf16 v[40:43], a[236:239], v[32:35], v[40:43]\n"
  "v_exp_f32 v254, v254\n"
  "v_mfma_f32_16x16x32_bf16 v[44:47], a[248:251], v[32:35], v[44:47]\n"
  "v_exp_f32 v24, v24\n"
  "v_mfma_f32_16x16x32_bf16 v[36:39], a[224:227], v[48:51], v[36:39]\n"
  "v_fmamk_f32 v25, v25, 0x3e553b95, v255\n"
  "v_fmamk_f32 v26, v26, 0x3e553b95, v255\n"
  "v_mfma_f32_16x16x32_bf16 v[40:43], a[240:243], v[48:51], v[40:43]\n"
  "s_waitcnt lgkmcnt(0)\n"
  "v_exp_f32 v25, v25\n"
  "v_mfma_f32_16x16x32_bf16 v[44:47], a[252:255], v[48:51], v[44:47]\n"
  "v_exp_f32 v26, v26\n"
  "v_mfma_f32_16x16x32_bf16 v[36:39], a[228:231], v[56:59], v[36:39]\n"
  "v_cvt_pk_bf16_f32 v24, v24, v25\n"
  "v_fmamk_f32 v27, v27, 0x3e553b95, v255\n"
  "v_mfma_f32_16x16x32_bf16 v[40:43], v[4:7], v[56:59], v[40:43]\n"
  "v_exp_f32 v27, v27\n"
  "v_mfma_f32_16x16x32_bf16 v[44:47], v[76:79], v[56:59], v[44:47]\n"
  "v_cvt_pk_bf16_f32 v25, v26, v27\n"
  "v_cvt_pk_bf16_f32 v26, v254, 0\n"
  "v_mfma_f32_16x16x32_bf16 v[20:23], v[200:203], v[108:111], 0\n"
  "v_mov_b32 v27, 0\n"
  "v_mfma_f32_16x16x32_bf16 v[32:35], v[72:75], v[108:111], 0\n"
  "v_mfma_f32_16x16x32_bf16 v[48:51], v[188:191], v[108:111], 0\n"
  "v_mfma_f32_16x16x32_bf16 v[12:15], v[16:19], v[124:127], v[12:15]\n"
  "s_nop 0\n"
  "s_nop 0\n"
  "v_mfma_f32_16x16x32_bf16 v[20:23], v[112:115], v[124:127], v[20:23]\n"
  "s_nop 0\n"
  "s_nop 0\n"
  "v_mfma_f32_16x16x32_bf16 v[32:35], v[8:11], v[124:127], v[32:35]\n"
  "s_nop 0\n"
  "s_nop 0\n"
  "v_mfma_f32_16x16x32_bf16 v[48:51], v[136:139], v[124:127], v[48:51]\n"
  "v_rcp_f32 v12, v12\n"
  "v_mfma_f32_16x16x32_bf16 v[56:59], v[16:19], v[156:159], 0\n"
  "v_mul_f32 v20, v20, v12\n"
  "v_mul_f32 v21, v21, v12\n"
  "v_mfma_f32_16x16x32_bf16 v[60:63], v[200:203], v[156:159], 0\n"
  "ds_read_b128 v[64:67], v252 offset:37888\n"
  "v_cvt_pk_bf16_f32 v20, v20, v21\n"
  "v_mfma_f32_16x16x32_bf16 v[104:107], v[72:75], v[156:159], 0\n"
  "v_mul_f32 v22, v22, v12\n"
  "v_mul_f32 v23, v23, v12\n"
  "v_mfma_f32_16x16x32_bf16 v[72:75], v[188:191], v[156:159], 0\n"
  "v_cvt_pk_bf16_f32 v21, v22, v23\n"
  "v_mul_f32 v32, v32, v12\n"
  "v_mfma_f32_16x16x32_bf16 v[56:59], v[16:19], v[24:27], v[56:59]\n"
  "ds_read_b128 v[108:111], v252 offset:38912\n"
  "v_mul_f32 v33, v33, v12\n"
  "v_mfma_f32_16x16x32_bf16 v[60:63], v[112:115], v[24:27], v[60:63]\n"
  "v_cvt_pk_bf16_f32 v22, v32, v33\n"
  "v_mul_f32 v34, v34, v12\n"
  "v_mfma_f32_16x16x32_bf16 v[104:107], v[8:11], v[24:27], v[104:107]\n"
  "s_waitcnt lgkmcnt(1)\n"
  "v_mul_f32 v35, v35, v12\n"
  "v_cvt_pk_bf16_f32 v23, v34, v35\n"
  "v_mfma_f32_16x16x32_bf16 v[72:75], v[136:139], v[24:27], v[72:75]\n"
  "ds_read_b128 v[8:11], v252 offset:39936\n"
  "ds_write_b128 v251, v[20:23] offset:46080\n"
  "v_mfma_f32_16x16x32_bf16 v[36:39], v[80:83], v[64:67], v[36:39]\n"
  "s_waitcnt lgkmcnt(2)\n"
  "v_rcp_f32 v56, v56\n"
  "v_mfma_f32_16x16x32_bf16 v[40:43], v[84:87], v[64:67], v[40:43]\n"
  "v_mul_f32 v48, v48, v12\n"
  "v_mul_f32 v49, v49, v12\n"
  "v_mfma_f32_16x16x32_bf16 v[44:47], v[88:91], v[64:67], v[44:47]\n"
  "ds_read_b128 v[16:19], v252 offset:40960\n"
  "v_cvt_pk_bf16_f32 v48, v48, v49\n"
  "v_mfma_f32_16x16x32_bf16 v[36:39], v[92:95], v[108:111], v[36:39]\n"
  "s_waitcnt lgkmcnt(2)\n"
  "v_mul_f32 v50, v50, v12\n"
  "v_mul_f32 v51, v51, v12\n"
  "v_mfma_f32_16x16x32_bf16 v[40:43], v[96:99], v[108:111], v[40:43]\n"
  "v_cvt_pk_bf16_f32 v49, v50, v51\n"
  "v_mul_f32 v60, v60, v56\n"
  "v_mfma_f32_16x16x32_bf16 v[44:47], v[100:103], v[108:111], v[44:47]\n"
  "ds_read_b128 v[12:15], v252 offset:41984\n"
  "ds_write_b64 v247, v[48:49] offset:46080\n"
  "v_mfma_f32_16x16x32_bf16 v[52:55], a[216:219], v[8:11], v[52:55]\n"
  "v_cvt_pk_bf16_f32 v36, v36, v37\n"
  "v_cvt_pk_bf16_f32 v37, v38, v39\n"
  "v_mfma_f32_16x16x32_bf16 v[28:31], a[232:235], v[8:11], v[28:31]\n"
  "s_waitcnt lgkmcnt(2)\n"
  "ds_write_b64 v244, v[36:37] offset:6144\n"
  "v_mfma_f32_16x16x32_bf16 v[140:143], a[244:247], v[8:11], v[140:143]\n"
  "v_cvt_pk_bf16_f32 v40, v40, v41\n"
  "v_cvt_pk_bf16_f32 v41, v42, v43\n"
  "v_mfma_f32_16x16x32_bf16 v[52:55], a[220:223], v[16:19], v[52:55]\n"
  "ds_write_b64 v244, v[40:41] offset:6272\n"
  "v_mfma_f32_16x16x32_bf16 v[28:31], a[236:239], v[16:19], v[28:31]\n"
  "s_waitcnt lgkmcnt(3)\n"
  "v_cvt_pk_bf16_f32 v44, v44, v45\n"
  "v_cvt_pk_bf16_f32 v45, v46, v47\n"
  "v_mfma_f32_16x16x32_bf16 v[140:143], a[248:251], v[16:19], v[140:143]\n"
  "ds_write_b64 v244, v[44:45] offset:6400\n"
  "v_mfma_f32_16x16x32_bf16 v[52:55], a[224:227], v[12:15], v[52:55]\n"
  "v_mul_f32 v61, v61, v56\n"
  "v_cvt_pk_bf16_f32 v60, v60, v61\n"
  "v_mfma_f32_16x16x32_bf16 v[28:31], a[240:243], v[12:15], v[28:31]\n"
  "ds_read_b128 v[8:11], v252 offset:43008\n"
  "ds_read_b128 v[16:19], v252 offset:44032\n"
  "v_mfma_f32_16x16x32_bf16 v[140:143], a[252:255], v[12:15], v[140:143]\n"
  "ds_read_b128 v[20:23], v252 offset:45056\n"
  "v_mul_f32 v62, v62, v56\n"
  "v_mul_f32 v63, v63, v56\n"
  "v_cvt_pk_bf16_f32 v61, v62, v63\n"
  "v_mul_f32 v104, v104, v56\n"
  "v_mul_f32 v105, v105, v56\n"
  "v_cvt_pk_bf16_f32 v62, v104, v105\n"
  "v_mul_f32 v106, v106, v56\n"
  "v_mul_f32 v107, v107, v56\n"
  "v_cvt_pk_bf16_f32 v63, v106, v107\n"
  "v_mul_f32 v72, v72, v56\n"
  "v_mul_f32 v73, v73, v56\n"
  "v_cvt_pk_bf16_f32 v72, v72, v73\n"
  "v_mul_f32 v74, v74, v56\n"
  "v_mul_f32 v75, v75, v56\n"
  "v_cvt_pk_bf16_f32 v73, v74, v75\n"
  "s_waitcnt lgkmcnt(2)\n"
  "v_mfma_f32_16x16x32_bf16 v[52:55], a[228:231], v[8:11], v[52:55]\n"
  "s_mov_b64 exec, s[68:69]\n"
  "ds_write_b128 v251, v[60:63] offset:27648\n"
  "ds_write_b64 v247, v[72:73] offset:27648\n"
  "s_mov_b64 exec, -1\n"
  "v_mfma_f32_16x16x32_bf16 v[28:31], v[4:7], v[8:11], v[28:31]\n"
  "s_waitcnt lgkmcnt(2)\n"
  "v_mfma_f32_16x16x32_bf16 v[140:143], v[76:79], v[8:11], v[140:143]\n"
  "v_mfma_f32_16x16x32_bf16 v[52:55], v[80:83], v[16:19], v[52:55]\n"
  "v_mfma_f32_16x16x32_bf16 v[28:31], v[84:87], v[16:19], v[28:31]\n"
  "v_mfma_f32_16x16x32_bf16 v[140:143], v[88:91], v[16:19], v[140:143]\n"
  "v_mfma_f32_16x16x32_bf16 v[52:55], v[92:95], v[20:23], v[52:55]\n"
  "s_nop 0\n"
  "s_nop 0\n"
  "v_mfma_f32_16x16x32_bf16 v[28:31], v[96:99], v[20:23], v[28:31]\n"
  "s_nop 0\n"
  "s_waitcnt lgkmcnt(0)\n"
  "s_barrier\n"
  "v_mfma_f32_16x16x32_bf16 v[140:143], v[100:103], v[20:23], v[140:143]\n"
  "ds_read_b128 v[8:11], v252 offset:46080\n"
  "ds_read_b128 v[12:15], v252 offset:47104\n"
  "ds_read_b128 v[16:19], v252 offset:48128\n"
  "v_cvt_pk_bf16_f32 v52, v52, v53\n"
  "v_cvt_pk_bf16_f32 v53, v54, v55\n"
  "v_cvt_pk_bf16_f32 v28, v28, v29\n"
  "v_cvt_pk_bf16_f32 v29, v30, v31\n"
  "s_nop 0\n"
  "v_cvt_pk_bf16_f32 v140, v140, v141\n"
  "v_cvt_pk_bf16_f32 v141, v142, v143\n"
  "ds_write_b64 v244, v[52:53] offset:12288\n"
  "ds_write_b64 v244, v[28:29] offset:12416\n"
  "s_waitcnt lgkmcnt(4)\n"
  "v_mfma_f32_16x16x32_bf16 v[164:167], a[216:219], v[8:11], v[164:167]\n"
  "ds_write_b64 v244, v[140:141] offset:12544\n"
  "v_mfma_f32_16x16x32_bf16 v[132:135], a[232:235], v[8:11], v[132:135]\n"
  "s_waitcnt lgkmcnt(3)\n"
  "v_mfma_f32_16x16x32_bf16 v[184:187], a[244:247], v[8:11], v[184:187]\n"
  "ds_read_b128 v[20:23], v252 offset:49152\n"
  "v_mfma_f32_16x16x32_bf16 v[164:167], a[220:223], v[12:15], v[164:167]\n"
  "ds_read_u16 v255, v243 offset:32\n"
  "ds_read_b128 v[8:11], v245 offset:0\n"
  "v_mfma_f32_16x16x32_bf16 v[132:135], a[236:239], v[12:15], v[132:135]\n"
  "ds_read_b128 v[24:27], v245 offset:128\n"
  "v_mfma_f32_16x16x32_bf16 v[184:187], a[248:251], v[12:15], v[184:187]\n"
  "ds_read_b128 v[28:31], v252 offset:50176\n"
  "v_mfma_f32_16x16x32_bf16 v[164:167], a[224:227], v[16:19], v[164:167]\n"
  "ds_read_b128 v[12:15], v245 offset:256\n"
  "v_mfma_f32_16x16x32_bf16 v[132:135], a[240:243], v[16:19], v[132:135]\n"
  "v_mfma_f32_16x16x32_bf16 v[184:187], a[252:255], v[16:19], v[184:187]\n"
  "s_waitcnt lgkmcnt(5)\n"
  "ds_read_b128 v[32:35], v252 offset:51200\n"
  "v_mfma_f32_16x16x32_bf16 v[164:167], a[228:231], v[20:23], v[164:167]\n"
  "s_waitcnt lgkmcnt(5)\n"
  "v_mad_u32_u24 v255, v255, s42, v242\n"
  "v_mfma_f32_16x16x32_bf16 v[132:135], v[4:7], v[20:23], v[132:135]\n"
  "s_waitcnt lgkmcnt(4)\n"
  "buffer_store_dwordx4 v[8:11], v255, s[24:27], s41 offen offset:0 sc1\n"
  "v_mfma_f32_16x16x32_bf16 v[184:187], v[76:79], v[20:23], v[184:187]\n"
  "s_waitcnt lgkmcnt(1)\n"
  "ds_read_b128 v[8:11], v252 offset:27648\n"
  "buffer_store_dwordx4 v[24:27], v255, s[24:27], s41 offen offset:128 sc1\n"
  "v_mfma_f32_16x16x32_bf16 v[164:167], v[80:83], v[28:31], v[164:167]\n"
  "s_waitcnt lgkmcnt(1)\n"
  "buffer_store_dwordx4 v[12:15], v255, s[24:27], s41 offen offset:256 sc1\n"
  "v_mfma_f32_16x16x32_bf16 v[132:135], v[84:87], v[28:31], v[132:135]\n"
  "s_waitcnt lgkmcnt(0)\n"
  "v_mfma_f32_16x16x32_bf16 v[184:187], v[88:91], v[28:31], v[184:187]\n"
  "ds_read_b128 v[12:15], v252 offset:28672\n"
  "v_mfma_f32_16x16x32_bf16 v[164:167], v[92:95], v[32:35], v[164:167]\n"
  "s_nop 0\n"
  "s_nop 0\n"
  "v_mfma_f32_16x16x32_bf16 v[132:135], v[96:99], v[32:35], v[132:135]\n"
  "s_nop 0\n"
  "s_nop 0\n"
  "v_mfma_f32_16x16x32_bf16 v[184:187], v[100:103], v[32:35], v[184:187]\n"
  "ds_read_b128 v[16:19], v252 offset:29696\n"
  "s_nop 0\n"
  "v_mfma_f32_16x16x32_bf16 v[116:119], a[216:219], v[8:11], v[116:119]\n"
  "v_cvt_pk_bf16_f32 v164, v164, v165\n"
  "v_cvt_pk_bf16_f32 v165, v166, v167\n"
  "v_mfma_f32_16x16x32_bf16 v[152:155], a[232:235], v[8:11], v[152:155]\n"
  "s_waitcnt lgkmcnt(1)\n"
  "ds_write_b64 v244, v[164:165] offset:18432\n"
  "v_mfma_f32_16x16x32_bf16 v[68:71], a[244:247], v[8:11], v[68:71]\n"
  "v_cvt_pk_bf16_f32 v132, v132, v133\n"
  "v_cvt_pk_bf16_f32 v133, v134, v135\n"
  "v_mfma_f32_16x16x32_bf16 v[116:119], a[220:223], v[12:15], v[116:119]\n"
  "ds_write_b64 v244, v[132:133] offset:18560\n"
  "v_mfma_f32_16x16x32_bf16 v[152:155], a[236:239], v[12:15], v[152:155]\n"
  "s_waitcnt lgkmcnt(2)\n"
  "v_cvt_pk_bf16_f32 v184, v184, v185\n"
  "v_cvt_pk_bf16_f32 v185, v186, v187\n"
  "v_mfma_f32_16x16x32_bf16 v[68:71], a[248:251], v[12:15], v[68:71]\n"
  "ds_write_b64 v244, v[184:185] offset:18688\n"
  "v_mfma_f32_16x16x32_bf16 v[116:119], a[224:227], v[16:19], v[116:119]\n"
  "ds_read_b128 v[8:11], v252 offset:30720\n"
  "ds_read_b128 v[12:15], v252 offset:31744\n"
  "v_mfma_f32_16x16x32_bf16 v[152:155], a[240:243], v[16:19], v[152:155]\n"
  "v_subrev_u32 v255, 52224, v244\n"
  "v_mfma_f32_16x16x32_bf16 v[68:71], a[252:255], v[16:19], v[68:71]\n"
  "ds_read_b128 v[20:23], v252 offset:32768\n"
  "v_subrev_u32 v254, 52224, v245\n"
  "s_waitcnt lgkmcnt(2)\n"
  "v_mfma_f32_16x16x32_bf16 v[116:119], a[228:231], v[8:11], v[116:119]\n"
  "v_mfma_f32_16x16x32_bf16 v[152:155], v[4:7], v[8:11], v[152:155]\n"
  "s_waitcnt lgkmcnt(1)\n"
  "v_mfma_f32_16x16x32_bf16 v[68:71], v[76:79], v[8:11], v[68:71]\n"
  "s_waitcnt lgkmcnt(0)\n"
  "v_mfma_f32_16x16x32_bf16 v[116:119], v[80:83], v[12:15], v[116:119]\n"
  "v_mfma_f32_16x16x32_bf16 v[152:155], v[84:87], v[12:15], v[152:155]\n"
  "v_mfma_f32_16x16x32_bf16 v[68:71], v[88:91], v[12:15], v[68:71]\n"
  "v_mfma_f32_16x16x32_bf16 v[116:119], v[92:95], v[20:23], v[116:119]\n"
  "s_nop 0\n"
  "s_nop 0\n"
  "v_mfma_f32_16x16x32_bf16 v[152:155], v[96:99], v[20:23], v[152:155]\n"
  "s_nop 0\n"
  "s_nop 0\n"
  "v_mfma_f32_16x16x32_bf16 v[68:71], v[100:103], v[20:23], v[68:71]\n"
  "s_nop 0\n"
  "s_nop 0\n"
  "v_cvt_pk_bf16_f32 v116, v116, v117\n"
  "v_cvt_pk_bf16_f32 v117, v118, v119\n"
  "s_nop 0\n"
  "ds_write_b64 v255, v[116:117] offset:0\n"
  "v_cvt_pk_bf16_f32 v152, v152, v153\n"
  "v_cvt_pk_bf16_f32 v153, v154, v155\n"
  "ds_write_b64 v255, v[152:153] offset:128\n"
  "v_cvt_pk_bf16_f32 v68, v68, v69\n"
  "v_cvt_pk_bf16_f32 v69, v70, v71\n"
  "ds_write_b64 v255, v[68:69] offset:256\n"
  "s_waitcnt lgkmcnt(0)\n"
  "s_barrier\n"
  "ds_read_u16 v255, v243 offset:40\n"
  "ds_read_b128 v[4:7], v245 offset:12288\n"
  "ds_read_b128 v[8:11], v245 offset:12416\n"
  "ds_read_b128 v[12:15], v245 offset:12544\n"
  "ds_read_u16 v253, v243 offset:48\n"
  "ds_read_b128 v[16:19], v254 offset:0\n"
  "ds_read_b128 v[20:23], v254 offset:128\n"
  "ds_read_b128 v[24:27], v254 offset:256\n"
  "s_waitcnt lgkmcnt(7)\n"
  "v_mad_u32_u24 v255, v255, s42, v242\n"
  "s_waitcnt lgkmcnt(6)\n"
  "buffer_store_dwordx4 v[4:7], v255, s[24:27], s41 offen offset:0 sc1\n"
  "s_waitcnt lgkmcnt(5)\n"
  "buffer_store_dwordx4 v[8:11], v255, s[24:27], s41 offen offset:128 sc1\n"
  "s_waitcnt lgkmcnt(4)\n"
  "buffer_store_dwordx4 v[12:15], v255, s[24:27], s41 offen offset:256 sc1\n"
  "s_waitcnt lgkmcnt(3)\n"
  "v_mad_u32_u24 v253, v253, s42, v242\n"
  "s_waitcnt lgkmcnt(2)\n"
  "buffer_store_dwordx4 v[16:19], v253, s[24:27], s41 offen offset:0 sc1\n"
  "s_waitcnt lgkmcnt(1)\n"
  "buffer_store_dwordx4 v[20:23], v253, s[24:27], s41 offen offset:128 sc1\n"
  "s_waitcnt lgkmcnt(0)\n"
  "buffer_store_dwordx4 v[24:27], v253, s[24:27], s41 offen offset:256 sc1\n"
  "91:\n"
  : "+v"(tid)
  : "n"(0), "s"(__builtin_amdgcn_kernarg_segment_ptr()), "n"(0), "n"(0), "n"(0), "n"(0), "s"(blockIdx.x), "s"((unsigned)pt), "s"((unsigned)(pt >> 32))
  : "v1","v2","v3","v4","v5","v6","v7","v8","v9","v10","v11","v12","v13","v14","v15","v16","v17","v18","v19","v20","v21","v22","v23","v24","v25","v26","v27","v28","v29","v30","v31","v32","v33","v34","v35","v36","v37","v38","v39","v40","v41","v42","v43","v44","v45","v46","v47","v48","v49","v50","v51","v52","v53","v54","v55","v56","v57","v58","v59","v60","v61","v62","v63","v64","v65","v66","v67","v68","v69","v70","v71","v72","v73","v74","v75","v76","v77","v78","v79","v80","v81","v82","v83","v84","v85","v86","v87","v88","v89","v90","v91","v92","v93","v94","v95","v96","v97","v98","v99","v100","v101","v102","v103","v104","v105","v106","v107","v108","v109","v110","v111","v112","v113","v114","v115","v116","v117","v118","v119","v120","v121","v122","v123","v124","v125","v126","v127","v128","v129","v130","v131","v132","v133","v134","v135","v136","v137","v138","v139","v140","v141","v142","v143","v144","v145","v146","v147","v148","v149","v150","v151","v152","v153","v154","v155","v156","v157","v158","v159","v160","v161","v162","v163","v164","v165","v166","v167","v168","v169","v170","v171","v172","v173","v174","v175","v176","v177","v178","v179","v180","v181","v182","v183","v184","v185","v186","v187","v188","v189","v190","v191","v192","v193","v194","v195","v196","v197","v198","v199","v200","v201","v202","v203","v204","v205","v206","v207","v208","v209","v210","v211","v212","v213","v214","v215","v216","v217","v218","v219","v220","v221","v222","v223","v224","v225","v226","v227","v228","v229","v230","v231","v232","v233","v234","v235","v236","v237","v238","v239","v240","v241","v242","v243","v244","v245","v246","v247","v248","v249","v250","v251","v252","v253","v254","v255","a0","a1","a2","a3","a4","a5","a6","a7","a8","a9","a10","a11","a12","a13","a14","a15","a16","a17","a18","a19","a20","a21","a22","a23","a24","a25","a26","a27","a28","a29","a30","a31","a32","a33","a34","a35","a36","a37","a38","a39","a40","a41","a42","a43","a44","a45","a46","a47","a48","a49","a50","a51","a52","a53","a54","a55","a56","a57","a58","a59","a60","a61","a62","a63","a64","a65","a66","a67","a68","a69","a70","a71","a72","a73","a74","a75","a76","a77","a78","a79","a80","a81","a82","a83","a84","a85","a86","a87","a88","a89","a90","a91","a92","a93","a94","a95","a96","a97","a98","a99","a100","a101","a102","a103","a104","a105","a106","a107","a108","a109","a110","a111","a112","a113","a114","a115","a116","a117","a118","a119","a120","a121","a122","a123","a124","a125","a126","a127","a128","a129","a130","a131","a132","a133","a134","a135","a136","a137","a138","a139","a140","a141","a142","a143","a144","a145","a146","a147","a148","a149","a150","a151","a152","a153","a154","a155","a156","a157","a158","a159","a160","a161","a162","a163","a164","a165","a166","a167","a168","a169","a170","a171","a172","a173","a174","a175","a176","a177","a178","a179","a180","a181","a182","a183","a184","a185","a186","a187","a188","a189","a190","a191","a192","a193","a194","a195","a196","a197","a198","a199","a200","a201","a202","a203","a204","a205","a206","a207","a208","a209","a210","a211","a212","a213","a214","a215","a216","a217","a218","a219","a220","a221","a222","a223","a224","a225","a226","a227","a228","a229","a230","a231","a232","a233","a234","a235","a236","a237","a238","a239","a240","a241","a242","a243","a244","a245","a246","a247","a248","a249","a250","a251","a252","a253","a254","a255","s4","s5","s6","s7","s8","s9","s10","s11","s12","s13","s14","s15","s20","s21","s22","s23","s24","s25","s26","s27","s28","s29","s30","s31","s32","s33","s34","s35","s36","s37","s38","s39","s40","s41","s42","s43","s44","s45","s46","s47","s48","s49","s50","s51","s52","s53","s54","s55","s56","s57","s58","s59","s60","s61","s62","s63","s64","s65","s66","s67","s68","s69","s70","s71","s72","s73","s74","s75","s76","s77","s78","s79","s80","s81","s82","s83","s84","s85","s86","s87","s88","s89","s90","s91","s92","s93","s94","s95","vcc","scc","memory");
}

extern "C" void kernel_launch(void* const* d_in, const int* in_sizes, int n_in, void* d_out, int out_size, void* d_ws, size_t ws_size,
                              hipStream_t stream) {
  davit_kernel<<<256, 256, 0, stream>>>((const char*)d_in[0], (const char*)d_in[1], (const char*)d_in[2], (const char*)d_in[3], (char*)d_out, (char*)d_ws);
}
```
